```python
import torch

def _softmax(x, axis=-1):
    return torch.softmax(x, dim=axis)
def _gelu(x):
    return torch.nn.functional.gelu(x, approximate="tanh")
def _silu(x):
    return torch.nn.functional.silu(x)
def _sigmoid(x):
    return torch.sigmoid(x)
def _layernorm(x, axis=-1, eps=1e-5):
    m = x.mean(dim=axis, keepdim=True)
    v = x.var(dim=axis, keepdim=True, unbiased=False)
    return (x - m) / torch.sqrt(v + eps)
def _scatter_add(tgt, idx, src, axis=0):
    return tgt.clone().index_add_(axis, idx, src)
def _segment_sum(src, seg, n):
    out = torch.zeros((n,) + src.shape[1:], dtype=src.dtype)
    return out.index_add_(0, seg, src)
def _pad(x, pw):
    flat = tuple(v for pair in reversed(pw) for v in pair)
    return torch.nn.functional.pad(x, flat)

kernel_name = 'standalone__mha_block_cached__near_ridge'

def setup_inputs():

    _w = (lambda a: (lambda t: t.to(torch.bfloat16) if t.is_floating_point() else t)(torch.as_tensor(a)))
    B, Sq, D = 8, 96, 512
    H, Sk, Dh = 32, 1024, 128
    return {
        "x": _w((lambda *s: torch.randn(*s, dtype=torch.bfloat16) * 0.8)(B, Sq, D)),
        "k_cache": _w((lambda *s: torch.randn(*s, dtype=torch.bfloat16) * 0.8)(B, H, Sk, Dh)),
        "v_cache": _w((lambda *s: torch.randn(*s, dtype=torch.bfloat16) * 0.05)(B, H, Sk, Dh)),
        "wq": _w((lambda *s: torch.randn(*s, dtype=torch.bfloat16) * 0.8)(D, H * Dh) * (D ** -0.5)),
        "wo": _w((lambda *s: torch.randn(*s, dtype=torch.bfloat16) * 0.05)(H * Dh, D) * ((H * Dh) ** -0.5)),
    }

def reference(x, k_cache, v_cache, wq, wo):
    B, Sq, D = x.shape
    H, Dh = 32, 128
    q = (lambda x, *d: x.permute(*d))((x @ wq).reshape(B, Sq, H, Dh), 0, 2, 1, 3)
    scores = torch.einsum("bhqd,bhkd->bhqk", q, k_cache) * (Dh ** -0.5)
    p = _softmax(scores, axis=-1)
    o = torch.einsum("bhqk,bhkd->bhqd", p, v_cache)
    return (lambda x, *d: x.permute(*d))(o, 0, 2, 1, 3).reshape(B, Sq, H * Dh) @ wo
```

```cpp
#include <hip/hip_runtime.h>
#include <hip/hip_bf16.h>

typedef __attribute__((ext_vector_type(8))) __bf16 bf16x8;
typedef __attribute__((ext_vector_type(4))) __bf16 bf16x4;
typedef __attribute__((ext_vector_type(2))) __bf16 bf16x2;
typedef __attribute__((ext_vector_type(16))) float f32x16;
typedef __attribute__((ext_vector_type(4))) float f32x4;
typedef __attribute__((ext_vector_type(2))) float f32x2;
typedef __attribute__((ext_vector_type(4))) unsigned u32x4;
#define LAS __attribute__((address_space(3)))
#define DI __device__ __forceinline__

#define INFL 2
#define DIAG 0

constexpr int NB = 8, SQ = 96, DM = 512, NH = 32, SK = 1024, DH = 128;
constexpr unsigned UNIT = 8192, RING = 15, W_UNIT0 = 3, CTRL = 19 * UNIT, LDS_BYTES = 160 * 1024;
constexpr unsigned C_LANDED = CTRL, C_WLAND = CTRL + 16, C_CONS = CTRL + 32;
constexpr float QSCALE = 0.12751743082459868f;
constexpr float THR = 8.0f;
constexpr int SRD3 = 0x00020000;

DI unsigned rfl(unsigned x) { return __builtin_amdgcn_readfirstlane(x); }
DI unsigned cvtpk(float lo, float hi) { f32x2 v = {lo, hi}; return __builtin_bit_cast(unsigned, __builtin_convertvector(v, bf16x2)); }
DI bf16x8 pack8(const f32x16& x, int o) { u32x4 w = {cvtpk(x[o], x[o + 1]), cvtpk(x[o + 2], x[o + 3]), cvtpk(x[o + 4], x[o + 5]), cvtpk(x[o + 6], x[o + 7])}; return __builtin_bit_cast(bf16x8, w); }
DI f32x16 mfma32(bf16x8 a, bf16x8 b, f32x16 c) { return __builtin_amdgcn_mfma_f32_32x32x16_bf16(a, b, c, 0, 0, 0); }
DI f32x4 mfma16(bf16x8 a, bf16x8 b, f32x4 c) { return __builtin_amdgcn_mfma_f32_16x16x32_bf16(a, b, c, 0, 0, 0); }
DI bf16x8 lds128(unsigned a) { return *(LAS const bf16x8*)(size_t)a; }
DI bf16x8 ldstr(unsigned a0, unsigned a1) {
  bf16x4 lo = __builtin_amdgcn_ds_read_tr16_b64_v4bf16((LAS bf16x4*)(size_t)a0), hi = __builtin_amdgcn_ds_read_tr16_b64_v4bf16((LAS bf16x4*)(size_t)a1);
  return __builtin_shufflevector(lo, hi, 0, 1, 2, 3, 4, 5, 6, 7);
}
DI float xhalf_max(float v) { auto r = __builtin_amdgcn_permlane32_swap(__float_as_uint(v), __float_as_uint(v), false, false); return fmaxf(__uint_as_float(r[0]), __uint_as_float(r[1])); }
DI float xhalf_sum(float v) { auto r = __builtin_amdgcn_permlane32_swap(__float_as_uint(v), __float_as_uint(v), false, false); return __uint_as_float(r[0]) + __uint_as_float(r[1]); }
DI void lds_put(unsigned addr, unsigned val) { asm volatile("ds_write_b32 %0, %1" :: "v"(addr), "v"(val) : "memory"); }
DI unsigned lds_get(unsigned addr) { unsigned r; asm volatile("ds_read_b32 %0, %1\n\ts_waitcnt lgkmcnt(0)" : "=v"(r) : "v"(addr) : "memory"); return rfl(r); }
DI unsigned lds_min3(unsigned addr) { u32x4 r; asm volatile("ds_read_b128 %0, %1\n\ts_waitcnt lgkmcnt(0)" : "=v"(r) : "v"(addr) : "memory"); return rfl(min(min(r[0], r[1]), r[2])); }
template <bool NT> DI void dma(unsigned m0v, unsigned voff, __amdgpu_buffer_rsrc_t srd, unsigned soff) {
  if constexpr (NT) asm volatile("s_mov_b32 m0, %0\n\ts_nop 0\n\tbuffer_load_dwordx4 %1, %2, %3 offen nt lds" :: "s"(m0v), "v"(voff), "s"(srd), "s"(soff) : "memory");
  else              asm volatile("s_mov_b32 m0, %0\n\ts_nop 0\n\tbuffer_load_dwordx4 %1, %2, %3 offen lds" :: "s"(m0v), "v"(voff), "s"(srd), "s"(soff) : "memory");
}
template <int N> DI void wait_vm() { asm volatile("s_waitcnt vmcnt(%0)" :: "i"(N) : "memory"); }
#define STAMP(i) do {} while (0)

__global__ __launch_bounds__(256, 1) void attn_kernel(const char* __restrict__ x, const char* __restrict__ kc, const char* __restrict__ vc,
                                                      const char* __restrict__ wq, char* __restrict__ ofrag, unsigned long long* __restrict__ stamps) {
  extern __shared__ __attribute__((aligned(16))) char lds[];
  const unsigned lane = threadIdx.x & 63, wid = rfl(threadIdx.x >> 6);
  const unsigned blk = blockIdx.x, head = 4 * (blk & 7) + ((blk >> 3) & 3), b = blk >> 5;
  const unsigned lb = (unsigned)(size_t)(LAS char*)lds;
  if (wid == 0) STAMP(0);
  if (threadIdx.x < 16) *(LAS unsigned*)(size_t)(lb + CTRL + 4 * threadIdx.x) = 0;

  if (wid == 3) {
    const size_t base = ((size_t)b * NH + head) * (SK * DH * 2);
    const __amdgpu_buffer_rsrc_t sk = __builtin_amdgcn_make_buffer_rsrc((void*)(kc + base), 0, SK * DH * 2, SRD3);
    const __amdgpu_buffer_rsrc_t sv = __builtin_amdgcn_make_buffer_rsrc((void*)(vc + base), 0, SK * DH * 2, SRD3);
    const unsigned pv = 256 * ((lane >> 2) & 7) + 64 * (lane >> 5) + 16 * (lane & 3);
    const unsigned pe = pv ^ (16 * ((lane >> 4) & 1)), po = pe ^ 32;
    unsigned slot = lb, freed = 0;
    auto item = [&](bool isk, unsigned tile, unsigned i) {
      const unsigned need = i < 3 ? 0 : (i < RING ? i - 2 : i + 2);
      while (freed < need) freed = lds_min3(lb + C_CONS);
#pragma unroll
      for (int p = 0; p < 8; ++p)
        dma<true>(slot + 1024 * p, isk ? ((p & 2) ? po : pe) : pv, isk ? sk : sv, tile * UNIT + 2048 * (p >> 1) + 128 * (p & 1));
      slot += UNIT; if (slot == lb + RING * UNIT) slot = lb;
      if (i >= INFL) { wait_vm<8 * INFL>(); lds_put(lb + C_LANDED, i - INFL + 1); }
    };
    item(true, 0, 0); item(true, 1, 1);
    asm volatile("s_waitcnt lgkmcnt(0)\n\ts_barrier" ::: "memory");
    for (unsigned t = 0; t < 30; ++t) { item(false, t, 2 * t + 2); item(true, t + 2, 2 * t + 3); }
    item(false, 30, 62); item(false, 31, 63);
    if constexpr (INFL > 3) { wait_vm<24>(); lds_put(lb + C_LANDED, 61); }
    if constexpr (INFL > 2) { wait_vm<16>(); lds_put(lb + C_LANDED, 62); }
    if constexpr (INFL > 1) { wait_vm<8>(); lds_put(lb + C_LANDED, 63); }
    wait_vm<0>(); lds_put(lb + C_LANDED, 64);
    STAMP(4);
    return;
  }

  const unsigned r = lane & 31, hh = lane >> 5;
  bf16x8 qf[8];
  {
    const __amdgpu_buffer_rsrc_t sx = __builtin_amdgcn_make_buffer_rsrc((void*)(x + (size_t)b * SQ * DM * 2), 0, SQ * DM * 2, SRD3);
    const __amdgpu_buffer_rsrc_t sw = __builtin_amdgcn_make_buffer_rsrc((void*)wq, 0, DM * NH * DH * 2, SRD3);
    const unsigned pw = 8192 * ((lane >> 2) & 7) + 64 * (lane >> 5) + 16 * (lane & 3);
    const unsigned px = (32 * wid + r) * 1024 + 16 * hh;
    u32x4 xf[32];
#pragma unroll
    for (int g = 0; g < 4; ++g) {
#pragma unroll
      for (int n = 0; n < 11; ++n) {
        const unsigned p = min(wid + 3 * n, 31u) + 32 * g;
        dma<false>(lb + W_UNIT0 * UNIT + 1024 * p, pw, sw, (p >> 1) * (8 * 8192) + 128 * (p & 1) + head * 256);
      }
#pragma unroll
      for (int s = 8 * g; s < 8 * g + 8; ++s)
        asm volatile("buffer_load_dwordx4 %0, %1, %2, 0 offen offset:%3" : "=v"(xf[s]) : "v"(px), "s"(sx), "i"(32 * s) : "memory");
    }
    asm volatile("s_waitcnt lgkmcnt(0)\n\ts_barrier" ::: "memory");
    const unsigned p2 = lane & 3, bw = lb + W_UNIT0 * UNIT + 8 * (((p2 & 1) << 1) | (p2 >> 1)) + 64 * ((lane >> 2) & 3) + 32 * ((lane >> 4) & 1) + 2048 * hh;
    f32x16 qa[4] = {};
#pragma unroll
    for (int g = 0; g < 4; ++g) {
      asm volatile("s_waitcnt vmcnt(%8)" : "+v"(xf[8 * g]), "+v"(xf[8 * g + 1]), "+v"(xf[8 * g + 2]), "+v"(xf[8 * g + 3]), "+v"(xf[8 * g + 4]), "+v"(xf[8 * g + 5]), "+v"(xf[8 * g + 6]), "+v"(xf[8 * g + 7])
                   : "i"(19 * (3 - g)) : "memory");
      lds_put(lb + C_WLAND + 4 * wid, g + 1);
      while (lds_min3(lb + C_WLAND) < (unsigned)(g + 1)) {}
      if (g == 0 && wid == 0) STAMP(1);
#pragma unroll
      for (int jj = 0; jj < 4; ++jj) {
        const int j = 4 * g + jj;
        bf16x8 wf[2][4];
#pragma unroll
        for (int s = 0; s < 2; ++s)
#pragma unroll
          for (int db = 0; db < 4; ++db) wf[s][db] = ldstr(bw + j * UNIT + 4096 * s + 512 * db, bw + j * UNIT + 4096 * s + 512 * db + 256);
        lds_put(lb + C_CONS + 4 * wid, j + 1);
#pragma unroll
        for (int s = 0; s < 2; ++s)
#pragma unroll
          for (int db = 0; db < 4; ++db) qa[db] = mfma32(wf[s][db], __builtin_bit_cast(bf16x8, xf[2 * j + s]), qa[db]);
      }
    }
#pragma unroll
    for (int db = 0; db < 4; ++db) { qa[db] *= QSCALE; qf[2 * db] = pack8(qa[db], 0); qf[2 * db + 1] = pack8(qa[db], 8); }
  }
  if (wid == 0) STAMP(2);

  const unsigned bke = 2048 * (r >> 3) + 64 * (r & 7) + 16 * (hh ^ ((r >> 2) & 3)), bko = bke ^ 32;
  const unsigned bv = 8 * (lane & 3) + 64 * ((lane >> 2) & 3) + 32 * ((lane >> 4) & 1) + 256 * hh;
  unsigned slot = lb, landed = 0, item = 0;
  auto acquire = [&]() { while (landed <= item) landed = lds_get(lb + C_LANDED); };
  auto advance = [&]() { ++item; slot += UNIT; if (slot == lb + RING * UNIT) slot = lb; };
  bf16x8 kf[8], vf[2][4];
  auto read_k = [&]() {
    acquire();
#pragma unroll
    for (int s = 0; s < 8; ++s) kf[s] = lds128(slot + ((s & 1) ? bko : bke) + 512 * (s >> 1));
    advance(); lds_put(lb + C_CONS + 4 * wid, 16 + item);
  };
  auto read_v = [&]() {
    acquire();
#pragma unroll
    for (int s = 0; s < 2; ++s)
#pragma unroll
      for (int db = 0; db < 4; ++db) vf[s][db] = ldstr(slot + bv + 4096 * s + 512 * db, slot + bv + 4096 * s + 512 * db + 2048);
    advance();
  };
  auto rowmax = [&](const f32x16& s) {
    float a = fmaxf(fmaxf(s[0], s[1]), s[2]), c = fmaxf(fmaxf(s[3], s[4]), s[5]);
    a = fmaxf(fmaxf(a, s[6]), s[7]); c = fmaxf(fmaxf(c, s[8]), s[9]);
    a = fmaxf(fmaxf(a, s[10]), s[11]); c = fmaxf(fmaxf(c, s[12]), s[13]);
    return fmaxf(fmaxf(a, s[14]), fmaxf(c, s[15]));
  };
  f32x16 o[4] = {}, sc = {}, negm;
  float l0 = 0.f, l1 = 0.f;
  read_k();
#pragma unroll
  for (int s = 0; s < 8; ++s) sc = mfma32(kf[s], qf[s], sc);
  read_k();
  { const float m = xhalf_max(rowmax(sc)); sc -= m;
#pragma unroll
    for (int i = 0; i < 16; ++i) negm[i] = -m; }

  auto step = [&](auto has_qk, auto has_k) {
    read_v();
    f32x16 sn = negm;
    if constexpr (decltype(has_qk)::value) {
#pragma unroll
      for (int s = 0; s < 8; ++s) sn = mfma32(kf[s], qf[s], sn);
    }
#pragma unroll
    for (int i = 0; i < 16; ++i) sc[i] = __builtin_amdgcn_exp2f(sc[i]);
#pragma unroll
    for (int i = 0; i < 16; i += 2) { l0 += sc[i]; l1 += sc[i + 1]; }
    const bf16x8 p0 = pack8(sc, 0), p1 = pack8(sc, 8);
    if constexpr (decltype(has_k)::value) read_k();
#pragma unroll
    for (int db = 0; db < 4; ++db) o[db] = mfma32(vf[0][db], p0, o[db]);
#pragma unroll
    for (int db = 0; db < 4; ++db) o[db] = mfma32(vf[1][db], p1, o[db]);
    if constexpr (decltype(has_qk)::value) {
      const float mx = rowmax(sn);
      if (__builtin_expect(__any(mx > THR), 0)) {
        const float d = fmaxf(xhalf_max(mx), 0.f), f = __builtin_amdgcn_exp2f(-d);
        sn -= d; negm -= d; l0 *= f; l1 *= f;
#pragma unroll
        for (int db = 0; db < 4; ++db) o[db] *= f;
      }
      sc = sn;
    }
  };
  for (int t = 0; t < 30; ++t) step(std::true_type{}, std::true_type{});
  step(std::true_type{}, std::false_type{});
  step(std::false_type{}, std::false_type{});
  if (wid == 0) STAMP(3);

  const float inv = 1.0f / xhalf_sum(l0 + l1);
  char* op = ofrag + ((size_t)((b * 6 + 2 * wid + (r >> 4)) * NH + head) * 4096) + ((r & 15) + 16 * hh) * 16;
#pragma unroll
  for (int db = 0; db < 4; ++db) {
    o[db] *= inv;
    *(bf16x8*)(op + db * 1024) = pack8(o[db], 0);
    *(bf16x8*)(op + db * 1024 + 512) = pack8(o[db], 8);
  }
  if (wid == 0) STAMP(5);
}

__global__ __launch_bounds__(256, 1) void oproj_kernel(const char* __restrict__ ofrag, const char* __restrict__ wo, char* __restrict__ out) {
  extern __shared__ __attribute__((aligned(16))) char lds[];
  const unsigned lane = threadIdx.x & 63, wid = rfl(threadIdx.x >> 6);
  const unsigned blk = blockIdx.x, xcd = blk & 7, j = blk >> 3;
  const unsigned b = 2 * (xcd >> 1) + (j >> 4), mh = (j >> 3) & 1, nt = 8 * (xcd & 1) + (j & 7);
  const unsigned lb = (unsigned)(size_t)(LAS char*)lds, stg = lb + wid * 32768;
  const __amdgpu_buffer_rsrc_t sw = __builtin_amdgcn_make_buffer_rsrc((void*)wo, 0, NH * DH * DM * 2, SRD3);
  const unsigned pw = 1024 * (lane >> 2) + 16 * (lane & 3);
  const unsigned g = lane >> 4, bw = stg + 64 * (16 * (g >> 1) + 4 * (g & 1) + ((lane >> 2) & 3)) + 16 * (lane & 3);
  const char* ap = ofrag + (size_t)((b * 6 + 3 * mh) * NH + 8 * wid) * 4096 + lane * 16;
  f32x4 acc[3][2] = {};
#pragma unroll
  for (int rd = 0; rd < 2; ++rd) {
#pragma unroll
    for (int p = 0; p < 32; ++p) dma<false>(stg + 1024 * p, pw, sw, ((8 * wid + 4 * rd) * DH + 16 * p) * 1024 + nt * 64);
    u32x4 af[3][16];
#pragma unroll
    for (int s = 0; s < 16; ++s)
#pragma unroll
      for (int m = 0; m < 3; ++m) af[m][s] = *(const u32x4*)(ap + (size_t)m * NH * 4096 + (rd * 16 + s) * 1024);
    wait_vm<0>();
#pragma unroll
    for (int s = 0; s < 16; ++s) {
      const bf16x8 w0 = ldstr(bw + 2048 * s, bw + 2048 * s + 512), w1 = ldstr(bw + 2048 * s + 8, bw + 2048 * s + 512 + 8);
#pragma unroll
      for (int m = 0; m < 3; ++m) {
        acc[m][0] = mfma16(w0, __builtin_bit_cast(bf16x8, af[m][s]), acc[m][0]);
        acc[m][1] = mfma16(w1, __builtin_bit_cast(bf16x8, af[m][s]), acc[m][1]);
      }
    }
    asm volatile("s_waitcnt lgkmcnt(0)" ::: "memory");
  }
  LAS f32x4* red = (LAS f32x4*)(size_t)(lb + 131072);
#pragma unroll
  for (int m = 0; m < 3; ++m) { red[(wid * 6 + 2 * m) * 64 + lane] = acc[m][0]; red[(wid * 6 + 2 * m + 1) * 64 + lane] = acc[m][1]; }
  __syncthreads();
  if (wid < 3) {
    f32x4 a = red[(2 * wid) * 64 + lane], c = red[(2 * wid + 1) * 64 + lane];
#pragma unroll
    for (int w = 1; w < 4; ++w) { a += red[(w * 6 + 2 * wid) * 64 + lane]; c += red[(w * 6 + 2 * wid + 1) * 64 + lane]; }
    u32x4 v = {cvtpk(a[0], a[1]), cvtpk(a[2], a[3]), cvtpk(c[0], c[1]), cvtpk(c[2], c[3])};
    *(u32x4*)(out + (size_t)(b * SQ + 48 * mh + 16 * wid + (lane & 15)) * (DM * 2) + nt * 64 + g * 16) = v;
  }
}

extern "C" void kernel_launch(void* const* d_in, const int* in_sizes, int n_in, void* d_out, int out_size, void* d_ws, size_t ws_size, hipStream_t stream) {
  (void)hipFuncSetAttribute((const void*)attn_kernel, hipFuncAttributeMaxDynamicSharedMemorySize, LDS_BYTES);
  (void)hipFuncSetAttribute((const void*)oproj_kernel, hipFuncAttributeMaxDynamicSharedMemorySize, LDS_BYTES);
  char* ofrag = (char*)d_ws;
  unsigned long long* stamps = (unsigned long long*)((char*)d_ws + ws_size - (1 << 20));
  attn_kernel<<<256, 256, LDS_BYTES, stream>>>((const char*)d_in[0], (const char*)d_in[1], (const char*)d_in[2], (const char*)d_in[3], ofrag, stamps);
  oproj_kernel<<<256, 256, LDS_BYTES, stream>>>(ofrag, (const char*)d_in[4], (char*)d_out);
}
```

```cpp
#define PROBE 0
#include <hip/hip_runtime.h>
#include <hip/hip_bf16.h>

typedef __attribute__((ext_vector_type(8))) __bf16 bf16x8;
typedef __attribute__((ext_vector_type(4))) __bf16 bf16x4;
typedef __attribute__((ext_vector_type(2))) __bf16 bf16x2;
typedef __attribute__((ext_vector_type(16))) float f32x16;
typedef __attribute__((ext_vector_type(4))) float f32x4;
typedef __attribute__((ext_vector_type(2))) float f32x2;
typedef __attribute__((ext_vector_type(4))) unsigned u32x4;
#define LAS __attribute__((address_space(3)))
#define DI __device__ __forceinline__

#define INFL 2
#define DIAG 0

constexpr int NB = 8, SQ = 96, DM = 512, NH = 32, SK = 1024, DH = 128;
constexpr unsigned UNIT = 8192, RING = 15, W_UNIT0 = 3, CTRL = 19 * UNIT, LDS_BYTES = 160 * 1024;
constexpr unsigned C_LANDED = CTRL, C_WLAND = CTRL + 16, C_CONS = CTRL + 32;
constexpr float QSCALE = 0.12751743082459868f;
constexpr float THR = 8.0f;
constexpr int SRD3 = 0x00020000;

DI unsigned rfl(unsigned x) { return __builtin_amdgcn_readfirstlane(x); }
DI unsigned cvtpk(float lo, float hi) { f32x2 v = {lo, hi}; return __builtin_bit_cast(unsigned, __builtin_convertvector(v, bf16x2)); }
DI bf16x8 pack8(const f32x16& x, int o) { u32x4 w = {cvtpk(x[o], x[o + 1]), cvtpk(x[o + 2], x[o + 3]), cvtpk(x[o + 4], x[o + 5]), cvtpk(x[o + 6], x[o + 7])}; return __builtin_bit_cast(bf16x8, w); }
DI f32x16 mfma32(bf16x8 a, bf16x8 b, f32x16 c) { return __builtin_amdgcn_mfma_f32_32x32x16_bf16(a, b, c, 0, 0, 0); }
DI f32x4 mfma16(bf16x8 a, bf16x8 b, f32x4 c) { return __builtin_amdgcn_mfma_f32_16x16x32_bf16(a, b, c, 0, 0, 0); }
DI bf16x8 lds128(unsigned a) { return *(LAS const bf16x8*)(size_t)a; }
DI bf16x8 ldstr(unsigned a0, unsigned a1) {
  bf16x4 lo = __builtin_amdgcn_ds_read_tr16_b64_v4bf16((LAS bf16x4*)(size_t)a0), hi = __builtin_amdgcn_ds_read_tr16_b64_v4bf16((LAS bf16x4*)(size_t)a1);
  return __builtin_shufflevector(lo, hi, 0, 1, 2, 3, 4, 5, 6, 7);
}
DI float xhalf_max(float v) { auto r = __builtin_amdgcn_permlane32_swap(__float_as_uint(v), __float_as_uint(v), false, false); return fmaxf(__uint_as_float(r[0]), __uint_as_float(r[1])); }
DI float xhalf_sum(float v) { auto r = __builtin_amdgcn_permlane32_swap(__float_as_uint(v), __float_as_uint(v), false, false); return __uint_as_float(r[0]) + __uint_as_float(r[1]); }
DI void lds_put(unsigned addr, unsigned val) { asm volatile("ds_write_b32 %0, %1" :: "v"(addr), "v"(val) : "memory"); }
DI unsigned lds_get(unsigned addr) { unsigned r; asm volatile("ds_read_b32 %0, %1\n\ts_waitcnt lgkmcnt(0)" : "=v"(r) : "v"(addr) : "memory"); return rfl(r); }
DI unsigned lds_min3(unsigned addr) { u32x4 r; asm volatile("ds_read_b128 %0, %1\n\ts_waitcnt lgkmcnt(0)" : "=v"(r) : "v"(addr) : "memory"); return rfl(min(min(r[0], r[1]), r[2])); }
template <bool NT> DI void dma(unsigned m0v, unsigned voff, __amdgpu_buffer_rsrc_t srd, unsigned soff) {
  if constexpr (NT) asm volatile("s_mov_b32 m0, %0\n\ts_nop 0\n\tbuffer_load_dwordx4 %1, %2, %3 offen nt lds" :: "s"(m0v), "v"(voff), "s"(srd), "s"(soff) : "memory");
  else              asm volatile("s_mov_b32 m0, %0\n\ts_nop 0\n\tbuffer_load_dwordx4 %1, %2, %3 offen lds" :: "s"(m0v), "v"(voff), "s"(srd), "s"(soff) : "memory");
}
template <int N> DI void wait_vm() { asm volatile("s_waitcnt vmcnt(%0)" :: "i"(N) : "memory"); }
#define STAMP(i) do {} while (0)

__global__ __launch_bounds__(256, 1) void attn_kernel(const char* __restrict__ x, const char* __restrict__ kc, const char* __restrict__ vc,
                                                      const char* __restrict__ wq, char* __restrict__ ofrag, unsigned long long* __restrict__ stamps) {
  extern __shared__ __attribute__((aligned(16))) char lds[];
  const unsigned lane = threadIdx.x & 63, wid = rfl(threadIdx.x >> 6);
  const unsigned blk = blockIdx.x, head = 4 * (blk & 7) + ((blk >> 3) & 3), b = blk >> 5;
  const unsigned lb = (unsigned)(size_t)(LAS char*)lds;
  if (wid == 0) STAMP(0);
  if (threadIdx.x < 16) *(LAS unsigned*)(size_t)(lb + CTRL + 4 * threadIdx.x) = 0;

  if (wid == 3) {
    const size_t base = ((size_t)b * NH + head) * (SK * DH * 2);
    const __amdgpu_buffer_rsrc_t sk = __builtin_amdgcn_make_buffer_rsrc((void*)(kc + base), 0, SK * DH * 2, SRD3);
    const __amdgpu_buffer_rsrc_t sv = __builtin_amdgcn_make_buffer_rsrc((void*)(vc + base), 0, SK * DH * 2, SRD3);
    const unsigned pv = 256 * ((lane >> 2) & 7) + 64 * (lane >> 5) + 16 * (lane & 3);
    const unsigned pe = pv ^ (16 * ((lane >> 4) & 1)), po = pe ^ 32;
    unsigned slot = lb, freed = 0;
    auto item = [&](bool isk, unsigned tile, unsigned i) {
      const unsigned need = i < 3 ? 0 : (i < RING ? i - 2 : i + 2);
      while (freed < need) freed = lds_min3(lb + C_CONS);
#pragma unroll
      for (int p = 0; p < 8; ++p)
        dma<true>(slot + 1024 * p, isk ? ((p & 2) ? po : pe) : pv, isk ? sk : sv, tile * UNIT + 2048 * (p >> 1) + 128 * (p & 1));
      slot += UNIT; if (slot == lb + RING * UNIT) slot = lb;
      if (i >= INFL) { wait_vm<8 * INFL>(); lds_put(lb + C_LANDED, i - INFL + 1); }
    };
    item(true, 0, 0); item(true, 1, 1);
    asm volatile("s_waitcnt lgkmcnt(0)\n\ts_barrier" ::: "memory");
    for (unsigned t = 0; t < 30; ++t) { item(false, t, 2 * t + 2); item(true, t + 2, 2 * t + 3); }
    item(false, 30, 62); item(false, 31, 63);
    if constexpr (INFL > 3) { wait_vm<24>(); lds_put(lb + C_LANDED, 61); }
    if constexpr (INFL > 2) { wait_vm<16>(); lds_put(lb + C_LANDED, 62); }
    if constexpr (INFL > 1) { wait_vm<8>(); lds_put(lb + C_LANDED, 63); }
    wait_vm<0>(); lds_put(lb + C_LANDED, 64);
    STAMP(4);
    return;
  }

  const unsigned r = lane & 31, hh = lane >> 5;
  bf16x8 qf[8];
  {
    const __amdgpu_buffer_rsrc_t sx = __builtin_amdgcn_make_buffer_rsrc((void*)(x + (size_t)b * SQ * DM * 2), 0, SQ * DM * 2, SRD3);
    const __amdgpu_buffer_rsrc_t sw = __builtin_amdgcn_make_buffer_rsrc((void*)wq, 0, DM * NH * DH * 2, SRD3);
    const unsigned pw = 8192 * ((lane >> 2) & 7) + 64 * (lane >> 5) + 16 * (lane & 3);
    const unsigned px = (32 * wid + r) * 1024 + 16 * hh;
    u32x4 xf[32];
#pragma unroll
    for (int g = 0; g < 4; ++g) {
#pragma unroll
      for (int n = 0; n < 11; ++n) {
        const unsigned p = min(wid + 3 * n, 31u) + 32 * g;
        dma<false>(lb + W_UNIT0 * UNIT + 1024 * p, pw, sw, (p >> 1) * (8 * 8192) + 128 * (p & 1) + head * 256);
      }
#pragma unroll
      for (int s = 8 * g; s < 8 * g + 8; ++s)
        asm volatile("buffer_load_dwordx4 %0, %1, %2, 0 offen offset:%3" : "=v"(xf[s]) : "v"(px), "s"(sx), "i"(32 * s) : "memory");
    }
    asm volatile("s_waitcnt lgkmcnt(0)\n\ts_barrier" ::: "memory");
    const unsigned p2 = lane & 3, bw = lb + W_UNIT0 * UNIT + 8 * (((p2 & 1) << 1) | (p2 >> 1)) + 64 * ((lane >> 2) & 3) + 32 * ((lane >> 4) & 1) + 2048 * hh;
    f32x16 qa[4] = {};
#pragma unroll
    for (int g = 0; g < 4; ++g) {
      asm volatile("s_waitcnt vmcnt(%8)" : "+v"(xf[8 * g]), "+v"(xf[8 * g + 1]), "+v"(xf[8 * g + 2]), "+v"(xf[8 * g + 3]), "+v"(xf[8 * g + 4]), "+v"(xf[8 * g + 5]), "+v"(xf[8 * g + 6]), "+v"(xf[8 * g + 7])
                   : "i"(19 * (3 - g)) : "memory");
      lds_put(lb + C_WLAND + 4 * wid, g + 1);
      while (lds_min3(lb + C_WLAND) < (unsigned)(g + 1)) {}
      if (g == 0 && wid == 0) STAMP(1);
#pragma unroll
      for (int jj = 0; jj < 4; ++jj) {
        const int j = 4 * g + jj;
        bf16x8 wf[2][4];
#pragma unroll
        for (int s = 0; s < 2; ++s)
#pragma unroll
          for (int db = 0; db < 4; ++db) wf[s][db] = ldstr(bw + j * UNIT + 4096 * s + 512 * db, bw + j * UNIT + 4096 * s + 512 * db + 256);
        lds_put(lb + C_CONS + 4 * wid, j + 1);
#pragma unroll
        for (int s = 0; s < 2; ++s)
#pragma unroll
          for (int db = 0; db < 4; ++db) qa[db] = mfma32(wf[s][db], __builtin_bit_cast(bf16x8, xf[2 * j + s]), qa[db]);
      }
    }
#pragma unroll
    for (int db = 0; db < 4; ++db) { qa[db] *= QSCALE; qf[2 * db] = pack8(qa[db], 0); qf[2 * db + 1] = pack8(qa[db], 8); }
  }
  if (wid == 0) STAMP(2);

  const unsigned bke = 2048 * (r >> 3) + 64 * (r & 7) + 16 * (hh ^ ((r >> 2) & 3)), bko = bke ^ 32;
  const unsigned bv = 8 * (lane & 3) + 64 * ((lane >> 2) & 3) + 32 * ((lane >> 4) & 1) + 256 * hh;
  unsigned slot = lb, landed = 0, item = 0;
  auto acquire = [&]() { while (landed <= item) landed = lds_get(lb + C_LANDED); };
  auto advance = [&]() { ++item; slot += UNIT; if (slot == lb + RING * UNIT) slot = lb; };
  bf16x8 kf[8], vf[2][4];
  auto read_k = [&]() {
    acquire();
#pragma unroll
    for (int s = 0; s < 8; ++s) kf[s] = lds128(slot + ((s & 1) ? bko : bke) + 512 * (s >> 1));
    advance(); lds_put(lb + C_CONS + 4 * wid, 16 + item);
  };
  auto read_v = [&]() {
    acquire();
#pragma unroll
    for (int s = 0; s < 2; ++s)
#pragma unroll
      for (int db = 0; db < 4; ++db) vf[s][db] = ldstr(slot + bv + 4096 * s + 512 * db, slot + bv + 4096 * s + 512 * db + 2048);
    advance();
  };
  auto rowmax = [&](const f32x16& s) {
    float a = fmaxf(fmaxf(s[0], s[1]), s[2]), c = fmaxf(fmaxf(s[3], s[4]), s[5]);
    a = fmaxf(fmaxf(a, s[6]), s[7]); c = fmaxf(fmaxf(c, s[8]), s[9]);
    a = fmaxf(fmaxf(a, s[10]), s[11]); c = fmaxf(fmaxf(c, s[12]), s[13]);
    return fmaxf(fmaxf(a, s[14]), fmaxf(c, s[15]));
  };
  f32x16 o[4] = {}, sc = {}, negm;
  float l0 = 0.f, l1 = 0.f;
  read_k();
#pragma unroll
  for (int s = 0; s < 8; ++s) sc = mfma32(kf[s], qf[s], sc);
  read_k();
  { const float m = xhalf_max(rowmax(sc)); sc -= m;
#pragma unroll
    for (int i = 0; i < 16; ++i) negm[i] = -m; }

  auto step = [&](auto has_qk, auto has_k) {
    read_v();
    f32x16 sn = negm;
    if constexpr (decltype(has_qk)::value) {
#pragma unroll
      for (int s = 0; s < 8; ++s) sn = mfma32(kf[s], qf[s], sn);
    }
#pragma unroll
    for (int i = 0; i < 16; ++i) sc[i] = __builtin_amdgcn_exp2f(sc[i]);
#pragma unroll
    for (int i = 0; i < 16; i += 2) { l0 += sc[i]; l1 += sc[i + 1]; }
    const bf16x8 p0 = pack8(sc, 0), p1 = pack8(sc, 8);
    if constexpr (decltype(has_k)::value) read_k();
#pragma unroll
    for (int db = 0; db < 4; ++db) o[db] = mfma32(vf[0][db], p0, o[db]);
#pragma unroll
    for (int db = 0; db < 4; ++db) o[db] = mfma32(vf[1][db], p1, o[db]);
    if constexpr (decltype(has_qk)::value) {
      const float mx = rowmax(sn);
      if (__builtin_expect(__any(mx > THR), 0)) {
        const float d = fmaxf(xhalf_max(mx), 0.f), f = __builtin_amdgcn_exp2f(-d);
        sn -= d; negm -= d; l0 *= f; l1 *= f;
#pragma unroll
        for (int db = 0; db < 4; ++db) o[db] *= f;
      }
      sc = sn;
    }
  };
  for (int t = 0; t < 30; ++t) step(std::true_type{}, std::true_type{});
  step(std::true_type{}, std::false_type{});
  step(std::false_type{}, std::false_type{});
  if (wid == 0) STAMP(3);

  const float inv = 1.0f / xhalf_sum(l0 + l1);
  char* op = ofrag + ((size_t)((b * 6 + 2 * wid + (r >> 4)) * NH + head) * 4096) + ((r & 15) + 16 * hh) * 16;
#pragma unroll
  for (int db = 0; db < 4; ++db) {
    o[db] *= inv;
    *(bf16x8*)(op + db * 1024) = pack8(o[db], 0);
    *(bf16x8*)(op + db * 1024 + 512) = pack8(o[db], 8);
  }
  if (wid == 0) STAMP(5);
}

__global__ __launch_bounds__(256, 1) void oproj_kernel(const char* __restrict__ ofrag, const char* __restrict__ wo, char* __restrict__ out) {
  extern __shared__ __attribute__((aligned(16))) char lds[];
  const unsigned lane = threadIdx.x & 63, wid = rfl(threadIdx.x >> 6);
  const unsigned blk = blockIdx.x, xcd = blk & 7, j = blk >> 3;
  const unsigned b = 2 * (xcd >> 1) + (j >> 4), mh = (j >> 3) & 1, nt = 8 * (xcd & 1) + (j & 7);
  const unsigned lb = (unsigned)(size_t)(LAS char*)lds, stg = lb + wid * 32768;
  const __amdgpu_buffer_rsrc_t sw = __builtin_amdgcn_make_buffer_rsrc((void*)wo, 0, NH * DH * DM * 2, SRD3);
  const unsigned pw = 1024 * (lane >> 2) + 16 * (lane & 3);
  const unsigned g = lane >> 4, bw = stg + 64 * (16 * (g >> 1) + 4 * (g & 1) + ((lane >> 2) & 3)) + 16 * (lane & 3);
  const char* ap = ofrag + (size_t)((b * 6 + 3 * mh) * NH + 8 * wid) * 4096 + lane * 16;
  f32x4 acc[3][2] = {};
#pragma unroll
  for (int rd = 0; rd < 2; ++rd) {
#pragma unroll
    for (int p = 0; p < 32; ++p) dma<false>(stg + 1024 * p, pw, sw, ((8 * wid + 4 * rd) * DH + 16 * p) * 1024 + nt * 64);
    u32x4 af[3][16];
#pragma unroll
    for (int s = 0; s < 16; ++s)
#pragma unroll
      for (int m = 0; m < 3; ++m) af[m][s] = *(const u32x4*)(ap + (size_t)m * NH * 4096 + (rd * 16 + s) * 1024);
    wait_vm<0>();
#pragma unroll
    for (int s = 0; s < 16; ++s) {
      const bf16x8 w0 = ldstr(bw + 2048 * s, bw + 2048 * s + 512), w1 = ldstr(bw + 2048 * s + 8, bw + 2048 * s + 512 + 8);
#pragma unroll
      for (int m = 0; m < 3; ++m) {
        acc[m][0] = mfma16(w0, __builtin_bit_cast(bf16x8, af[m][s]), acc[m][0]);
        acc[m][1] = mfma16(w1, __builtin_bit_cast(bf16x8, af[m][s]), acc[m][1]);
      }
    }
    asm volatile("s_waitcnt lgkmcnt(0)" ::: "memory");
  }
  LAS f32x4* red = (LAS f32x4*)(size_t)(lb + 131072);
#pragma unroll
  for (int m = 0; m < 3; ++m) { red[(wid * 6 + 2 * m) * 64 + lane] = acc[m][0]; red[(wid * 6 + 2 * m + 1) * 64 + lane] = acc[m][1]; }
  __syncthreads();
  if (wid < 3) {
    f32x4 a = red[(2 * wid) * 64 + lane], c = red[(2 * wid + 1) * 64 + lane];
#pragma unroll
    for (int w = 1; w < 4; ++w) { a += red[(w * 6 + 2 * wid) * 64 + lane]; c += red[(w * 6 + 2 * wid + 1) * 64 + lane]; }
    u32x4 v = {cvtpk(a[0], a[1]), cvtpk(a[2], a[3]), cvtpk(c[0], c[1]), cvtpk(c[2], c[3])};
    *(u32x4*)(out + (size_t)(b * SQ + 48 * mh + 16 * wid + (lane & 15)) * (DM * 2) + nt * 64 + g * 16) = v;
  }
}

constexpr int AMP = 8;
__global__ __launch_bounds__(256, 1) void probe_kernel(const char* __restrict__ kc, const char* __restrict__ vc, const char* __restrict__ wq, unsigned* __restrict__ sink) {
  extern __shared__ __attribute__((aligned(16))) char lds[];
  const unsigned lane = threadIdx.x & 63, wid = rfl(threadIdx.x >> 6), blk = blockIdx.x;
  if (wid != 0) return;
  const unsigned long long t0 = __builtin_amdgcn_s_memrealtime();
  unsigned long long dur = 0;
  const __amdgpu_buffer_rsrc_t sk = __builtin_amdgcn_make_buffer_rsrc((void*)(kc + (size_t)blk * 262144), 0, 262144, SRD3);
  const __amdgpu_buffer_rsrc_t sv = __builtin_amdgcn_make_buffer_rsrc((void*)(vc + (size_t)blk * 262144), 0, 262144, SRD3);
  if (PROBE == 1 || PROBE == 2) {
    for (unsigned i = 0; i < 64; ++i) {
#pragma unroll
      for (int p = 0; p < 8; ++p) dma<PROBE == 1>((i & 7) * 8192 + 1024 * p, lane * 16, (i & 1) ? sv : sk, (i >> 1) * 8192 + 1024 * p);
      wait_vm<16>();
    }
    wait_vm<0>();
    dur = __builtin_amdgcn_s_memrealtime() - t0;
  }
  if (PROBE == 3) { dma<true>(0, lane * 16, sk, 0); wait_vm<0>(); dur = __builtin_amdgcn_s_memrealtime() - t0; }
  if (PROBE == 4) { const unsigned long long c0 = __builtin_amdgcn_s_memtime(); while (__builtin_amdgcn_s_memrealtime() - t0 < 500) {} dur = (__builtin_amdgcn_s_memtime() - c0) / 24; }
  if (PROBE == 5) { asm volatile(".rept 4096\n\ts_nop 0\n\t.endr" ::: "memory"); dur = __builtin_amdgcn_s_memrealtime() - t0; }
  if (PROBE == 6) { dma<true>(0, lane * 16, sk, 0); wait_vm<0>(); const unsigned long long t1 = __builtin_amdgcn_s_memrealtime(); dma<true>(0, lane * 16, sk, 131072); wait_vm<0>(); dur = __builtin_amdgcn_s_memrealtime() - t1; }
  while (__builtin_amdgcn_s_memrealtime() - t0 < AMP * dur) __builtin_amdgcn_s_sleep(2);
  if (dur == 0x123456789ull) sink[0] = 1;
}
extern "C" void kernel_launch(void* const* d_in, const int* in_sizes, int n_in, void* d_out, int out_size, void* d_ws, size_t ws_size, hipStream_t stream) {
  (void)hipFuncSetAttribute((const void*)attn_kernel, hipFuncAttributeMaxDynamicSharedMemorySize, LDS_BYTES);
  (void)hipFuncSetAttribute((const void*)oproj_kernel, hipFuncAttributeMaxDynamicSharedMemorySize, LDS_BYTES);
  char* ofrag = (char*)d_ws;
  unsigned long long* stamps = (unsigned long long*)((char*)d_ws + ws_size - (1 << 20));
  (void)hipFuncSetAttribute((const void*)probe_kernel, hipFuncAttributeMaxDynamicSharedMemorySize, LDS_BYTES);
  probe_kernel<<<256, 256, LDS_BYTES, stream>>>((const char*)d_in[1], (const char*)d_in[2], (const char*)d_in[3], (unsigned*)((char*)d_ws + (64 << 20)));
  attn_kernel<<<256, 256, LDS_BYTES, stream>>>((const char*)d_in[0], (const char*)d_in[1], (const char*)d_in[2], (const char*)d_in[3], ofrag, stamps);
  oproj_kernel<<<256, 256, LDS_BYTES, stream>>>(ofrag, (const char*)d_in[4], (char*)d_out);
}
```

```cpp
#include <hip/hip_runtime.h>
#include <hip/hip_bf16.h>

constexpr unsigned LDS_BYTES = 160 * 1024;

__global__ __launch_bounds__(256, 1) void mha_block_kernel(const void* x, const void* k, const void* v, const void* wq, const void* wo,
                                                           void* out, void* ws, void* aux) {
  unsigned tid = threadIdx.x;
  asm volatile(
    "s_load_dwordx16 s[20:35], %[karg], 0\n"
    "s_mov_b32 s36, %[blk]\n"
    "v_lshrrev_b32 v235, 6, %[tid]\n"
    "v_and_b32 v224, 63, %[tid]\n"
    "v_readfirstlane_b32 s37, v235\n"
    "v_mov_b32 v225, 163776\n"
    "v_mov_b64 v[252:253], 0\n"
    "v_mov_b64 v[254:255], 0\n"
    "v_lshlrev_b32 v239, 4, v224\n"
    "v_add_u32 v236, v239, v225\n"
    "s_mov_b64 exec, 15\n"
    "ds_write_b128 v236, v[252:255]\n"
    "s_mov_b64 exec, -1\n"
    "s_and_b32 s83, s36, 7\n"
    "s_lshl_b32 s83, s83, 2\n"
    "s_bfe_u32 s60, s36, 0x20003\n"
    "s_or_b32 s83, s83, s60\n"
    "s_and_b32 s38, s36, 31\n"
    "s_lshr_b32 s39, s36, 5\n"
    "s_lshr_b32 s89, s83, 3\n"
    "s_and_b32 s91, s83, 7\n"
    "s_mov_b32 s79, 0x100000\n"
    "s_mov_b32 s72, 0\n"
    "s_mov_b32 s77, 0x1ffff\n"
    "s_mov_b32 s43, 0x20000\n"
    "s_mov_b32 s54, 14155776\n"
    "s_mov_b32 s55, 0x20000\n"
    "v_bfe_u32 v235, v224, 2, 3\n"
    "v_lshrrev_b32 v236, 5, v224\n"
    "v_and_b32 v237, 3, v224\n"
    "v_lshlrev_b32 v226, 2, v224\n"
    "v_add_u32 v226, 161792, v226\n"
    "s_cmp_eq_u32 s37, 3\n"
    "s_cbranch_scc0 .Lmha_COMPUTE\n"
    ".Lmha_LOADER:\n"
    "s_mov_b64 exec, 1\n"
    "v_mov_b32 v226, v225\n"
    "s_mov_b64 exec, -1\n"
    "v_lshlrev_b32 v238, 4, v237\n"
    "v_lshl_or_b32 v238, v236, 6, v238\n"
    "v_lshl_or_b32 v230, v235, 13, v238\n"
    "v_mov_b32 v240, 4294967290\n"
    "s_lshl_b32 s64, s38, 8\n"
    "s_mov_b32 s42, 0x400000\n"
    "s_mov_b32 m0, 0\n"
    "s_mov_b32 s73, 0\n"
    "s_waitcnt lgkmcnt(0)\n"
    "s_barrier\n"
    "s_mov_b32 s40, s26\n"
    "s_and_b32 s41, s27, 0xffff\n"
    ".Lmha_LW:\n"
    "v_add_u32 v240, 1, v240\n"
    "s_waitcnt vmcnt(47)\n"
    "buffer_load_dwordx4 v230, s[40:43], s64 offen offset:0 lds\n"
    "s_add_u32 m0, m0, 896\n"
    "s_waitcnt vmcnt(47)\n"
    "buffer_load_dwordx4 v230, s[40:43], s64 offen offset:128 lds\n"
    "s_add_u32 m0, m0, 1152\n"
    "s_add_u32 s64, s64, 0x10000\n"
    "s_waitcnt vmcnt(47)\n"
    "buffer_load_dwordx4 v230, s[40:43], s64 offen offset:0 lds\n"
    "s_add_u32 m0, m0, 896\n"
    "s_waitcnt vmcnt(47)\n"
    "buffer_load_dwordx4 v230, s[40:43], s64 offen offset:128 lds\n"
    "s_add_u32 m0, m0, 1152\n"
    "s_add_u32 s64, s64, 0x10000\n"
    "s_waitcnt vmcnt(47)\n"
    "buffer_load_dwordx4 v230, s[40:43], s64 offen offset:0 lds\n"
    "s_add_u32 m0, m0, 896\n"
    "s_waitcnt vmcnt(47)\n"
    "buffer_load_dwordx4 v230, s[40:43], s64 offen offset:128 lds\n"
    "s_add_u32 m0, m0, 1152\n"
    "s_add_u32 s64, s64, 0x10000\n"
    "s_waitcnt vmcnt(47)\n"
    "buffer_load_dwordx4 v230, s[40:43], s64 offen offset:0 lds\n"
    "s_add_u32 m0, m0, 896\n"
    "s_waitcnt vmcnt(47)\n"
    "buffer_load_dwordx4 v230, s[40:43], s64 offen offset:128 lds\n"
    "s_add_u32 m0, m0, 1152\n"
    "s_add_u32 s64, s64, 0x10000\n"
    "v_max_i32 v252, 0, v240\n"
    "s_add_u32 s73, s73, 1\n"
    "s_cmp_lt_u32 s73, 16\n"
    "ds_write_b32 v226, v252 offset:0\n"
    "s_cbranch_scc1 .Lmha_LW\n"
    "s_branch .Lmha_LOADER2\n"
    "s_nop 0\n"
    "s_nop 0\n"
    "s_nop 0\n"
    "s_nop 0\n"
    ".Lmha_COMPUTE:\n"
    "s_lshl_b32 s60, s37, 2\n"
    "s_add_u32 s60, s60, 163776\n"
    "s_mov_b64 exec, 1\n"
    "v_mov_b32 v226, s60\n"
    "s_mov_b64 exec, -1\n"
    "s_lshl_b32 s76, s37, 13\n"
    "s_add_u32 s76, s76, 131072\n"
    "v_lshrrev_b32 v238, 3, v224\n"
    "v_and_b32 v246, 7, v224\n"
    "v_lshrrev_b32 v248, 4, v224\n"
    "v_xor_b32 v246, v246, v248\n"
    "v_lshlrev_b32 v246, 4, v246\n"
    "v_lshl_or_b32 v246, v238, 10, v246\n"
    "v_xor_b32 v247, 64, v246\n"
    "s_lshl_b32 s60, s37, 15\n"
    "v_add_u32 v246, s60, v246\n"
    "s_add_u32 s60, s60, 8192\n"
    "v_add_u32 v247, s60, v247\n"
    "v_add_u32 v233, 0x4000, v246\n"
    "v_add_u32 v234, 0x4000, v247\n"
    "s_mul_i32 s60, s39, 0x18000\n"
    "s_mov_b32 s42, 0x18000\n"
    "s_waitcnt lgkmcnt(0)\n"
    "s_barrier\n"
    "s_add_u32 s40, s20, s60\n"
    "s_addc_u32 s41, s21, 0\n"
    "s_and_b32 s41, s41, 0xffff\n"
    "s_add_u32 m0, s76, 0\n"
    "s_nop 0\n"
    "buffer_load_dwordx4 v246, s[40:43], 0 offen offset:0 lds\n"
    "s_add_u32 m0, s76, 1024\n"
    "s_nop 0\n"
    "buffer_load_dwordx4 v247, s[40:43], 0 offen offset:0 lds\n"
    "s_add_u32 m0, s76, 2048\n"
    "s_nop 0\n"
    "buffer_load_dwordx4 v233, s[40:43], 0 offen offset:0 lds\n"
    "s_add_u32 m0, s76, 3072\n"
    "s_nop 0\n"
    "buffer_load_dwordx4 v234, s[40:43], 0 offen offset:0 lds\n"
    "s_mov_b32 m0, 162304\n"
    "s_mov_b32 s44, s20\n"
    "s_and_b32 s45, s21, 0xffff\n"
    "s_mov_b32 s46, 0xc0000\n"
    "s_mov_b32 s47, 0x20000\n"
    "s_mul_i32 s60, s36, 3\n"
    "s_add_u32 s60, s60, s37\n"
    "s_lshl_b32 s60, s60, 10\n"
    "buffer_load_dwordx4 v239, s[44:47], s60 offen lds\n"
    "s_mov_b32 s44, s26\n"
    "s_and_b32 s45, s27, 0xffff\n"
    "s_mov_b32 s46, 0x400000\n"
    "s_mov_b32 s47, 0x20000\n"
    "s_lshl_b32 s60, s36, 4\n"
    "s_add_u32 s60, s60, s37\n"
    "s_lshl_b32 s60, s60, 10\n"
    "buffer_load_dwordx4 v239, s[44:47], s60 offen lds\n"
    "s_add_u32 s60, s60, 0xc00\n"
    "buffer_load_dwordx4 v239, s[44:47], s60 offen lds\n"
    "s_add_u32 s60, s60, 0xc00\n"
    "buffer_load_dwordx4 v239, s[44:47], s60 offen lds\n"
    "s_add_u32 s60, s60, 0xc00\n"
    "buffer_load_dwordx4 v239, s[44:47], s60 offen lds\n"
    "s_add_u32 s60, s60, 0xc00\n"
    "buffer_load_dwordx4 v239, s[44:47], s60 offen lds\n"
    "s_cmp_eq_u32 s37, 0\n"
    "s_cselect_b32 s61, 0xc00, 0\n"
    "s_add_u32 s60, s60, s61\n"
    "buffer_load_dwordx4 v239, s[44:47], s60 offen lds\n"
    "s_add_u32 m0, s76, 3968\n"
    "s_nop 0\n"
    "buffer_load_dwordx4 v246, s[40:43], 0 offen offset:128 lds\n"
    "s_add_u32 m0, s76, 4992\n"
    "s_nop 0\n"
    "buffer_load_dwordx4 v247, s[40:43], 0 offen offset:128 lds\n"
    "s_add_u32 m0, s76, 6016\n"
    "s_nop 0\n"
    "buffer_load_dwordx4 v233, s[40:43], 0 offen offset:128 lds\n"
    "s_add_u32 m0, s76, 7040\n"
    "s_nop 0\n"
    "buffer_load_dwordx4 v234, s[40:43], 0 offen offset:128 lds\n"
    "s_mov_b32 s52, s32\n"
    "s_and_b32 s53, s33, 0xffff\n"
    "s_mov_b32 s56, s30\n"
    "s_and_b32 s57, s31, 0xffff\n"
    "s_mov_b32 s58, 0xc0000\n"
    "s_mov_b32 s59, 0x20000\n"
    "s_mul_i32 s90, s39, 3\n"
    "s_add_u32 s90, s90, s37\n"
    "s_lshl_b32 s60, s90, 18\n"
    "s_add_u32 s60, s60, 262144\n"
    "s_lshl_b32 s61, s83, 13\n"
    "s_add_u32 s94, s60, s61\n"
    "s_lshl_b32 s61, s89, 16\n"
    "s_add_u32 s82, s60, s61\n"
    "s_lshl_b32 s60, s90, 11\n"
    "s_add_u32 s60, s60, 4096\n"
    "s_lshl_b32 s61, s83, 6\n"
    "s_add_u32 s95, s60, s61\n"
    "s_mov_b32 s88, s60\n"
    "s_lshl_b32 s62, s90, 3\n"
    "s_add_u32 s62, s62, s91\n"
    "s_lshl_b32 s62, s62, 15\n"
    "s_add_u32 s62, s62, 7340032\n"
    "s_lshl_b32 s61, s89, 13\n"
    "s_add_u32 s67, s62, s61\n"
    "s_add_u32 s60, s89, 1\n"
    "s_and_b32 s60, s60, 3\n"
    "s_lshl_b32 s60, s60, 13\n"
    "s_add_u32 s60, s60, s62\n"
    "s_add_u32 s64, s60, 2048\n"
    "s_add_u32 s60, s89, 2\n"
    "s_and_b32 s60, s60, 3\n"
    "s_lshl_b32 s60, s60, 13\n"
    "s_add_u32 s60, s60, s62\n"
    "s_add_u32 s65, s60, 4096\n"
    "s_add_u32 s60, s89, 3\n"
    "s_and_b32 s60, s60, 3\n"
    "s_lshl_b32 s60, s60, 13\n"
    "s_add_u32 s60, s60, s62\n"
    "s_add_u32 s66, s60, 6144\n"
    "s_lshl_b32 s60, s90, 15\n"
    "s_lshl_b32 s61, s91, 7\n"
    "s_add_u32 s60, s60, s61\n"
    "s_lshl_b32 s61, s89, 13\n"
    "s_add_u32 s71, s60, s61\n"
    "v_and_b32 v244, 7, v224\n"
    "v_min_u32 v245, 2, v244\n"
    "v_min_u32 v244, 6, v244\n"
    "v_add_u32 v244, 1, v244\n"
    "v_xor_b32 v244, s83, v244\n"
    "v_add_u32 v245, 1, v245\n"
    "v_lshlrev_b32 v245, 3, v245\n"
    "v_add_u32 v245, s83, v245\n"
    "v_and_b32 v245, 31, v245\n"
    "s_mov_b32 exec_lo, 0xff00ff00\n"
    "s_mov_b32 exec_hi, 0xff00ff00\n"
    "v_mov_b32 v244, v245\n"
    "s_mov_b64 exec, -1\n"
    "v_lshlrev_b32 v244, 6, v244\n"
    "s_lshl_b32 s60, s89, 3\n"
    "v_add_u32 v228, s60, v224\n"
    "v_and_b32 v228, 31, v228\n"
    "v_bfe_u32 v248, v228, 1, 3\n"
    "v_xor_b32 v248, v248, v236\n"
    "v_lshlrev_b32 v248, 4, v248\n"
    "v_lshl_or_b32 v248, v228, 7, v248\n"
    "v_add_u32 v248, s76, v248\n"
    "v_xor_b32 v249, 32, v248\n"
    "v_xor_b32 v250, 64, v248\n"
    "v_xor_b32 v251, 0x60, v248\n"
    "v_lshrrev_b32 v241, 1, v237\n"
    "v_and_b32 v229, 1, v237\n"
    "v_lshl_or_b32 v241, v229, 1, v241\n"
    "v_lshlrev_b32 v241, 3, v241\n"
    "v_bfe_u32 v229, v224, 2, 2\n"
    "v_lshlrev_b32 v229, 6, v229\n"
    "v_bfe_u32 v228, v224, 4, 1\n"
    "v_lshl_or_b32 v229, v228, 5, v229\n"
    "v_or_b32 v241, v241, v229\n"
    "v_lshl_or_b32 v241, v236, 11, v241\n"
    "v_add_u32 v232, 0x10000, v241\n"
    "v_lshl_or_b32 v229, v237, 3, v229\n"
    "v_lshl_or_b32 v229, v236, 8, v229\n"
    "v_bfe_u32 v227, v224, 2, 2\n"
    "v_xor_b32 v227, v227, v236\n"
    "v_lshlrev_b32 v227, 4, v227\n"
    "v_and_b32 v228, 7, v224\n"
    "v_lshl_or_b32 v227, v228, 6, v227\n"
    "v_bfe_u32 v228, v224, 3, 2\n"
    "v_lshl_or_b32 v227, v228, 11, v227\n"
    "v_xor_b32 v228, 32, v227\n"
    "s_mov_b32 s92, 1040356334\n"
    "s_mov_b32 s78, 1090519040\n"
    "s_mov_b32 s18, 0\n"
    "v_mov_b32 v240, 1\n"
    "s_getpc_b64 s[96:97]\n"
    ".Lmha_PC1:\n"
    "s_add_u32 s96, s96, .Lmha_TSPIN-.Lmha_PC1\n"
    "s_addc_u32 s97, s97, 0\n"
    "s_waitcnt vmcnt(11)\n"
    "ds_read_b128 v[64:67], v248 offset:0\n"
    "ds_read_b128 v[68:71], v249 offset:0\n"
    "ds_read_b128 v[72:75], v250 offset:0\n"
    "ds_read_b128 v[76:79], v251 offset:0\n"
    "s_waitcnt lgkmcnt(0)\n"
    "s_add_u32 m0, s76, 4294967040\n"
    "s_nop 0\n"
    "buffer_load_dwordx4 v246, s[40:43], 0 offen offset:256 lds\n"
    "s_add_u32 m0, s76, 768\n"
    "s_nop 0\n"
    "buffer_load_dwordx4 v247, s[40:43], 0 offen offset:256 lds\n"
    "s_add_u32 m0, s76, 1792\n"
    "s_nop 0\n"
    "buffer_load_dwordx4 v233, s[40:43], 0 offen offset:256 lds\n"
    "s_add_u32 m0, s76, 2816\n"
    "s_nop 0\n"
    "buffer_load_dwordx4 v234, s[40:43], 0 offen offset:256 lds\n"
    "s_mov_b32 s74, 2\n"
    "s_cmp_ge_u32 s72, s74\n"
    "s_cbranch_scc0 .Lmha_QW0S\n"
    ".Lmha_QW0:\n"
    "ds_read_b64_tr_b16 v[192:193], v241 offset:0\n"
    "ds_read_b64_tr_b16 v[194:195], v241 offset:256\n"
    "ds_read_b64_tr_b16 v[196:197], v241 offset:512\n"
    "ds_read_b64_tr_b16 v[198:199], v241 offset:768\n"
    "ds_read_b64_tr_b16 v[200:201], v241 offset:1024\n"
    "ds_read_b64_tr_b16 v[202:203], v241 offset:1280\n"
    "ds_read_b64_tr_b16 v[204:205], v241 offset:1536\n"
    "ds_read_b64_tr_b16 v[206:207], v241 offset:1792\n"
    "ds_read_b64_tr_b16 v[208:209], v241 offset:4096\n"
    "ds_read_b64_tr_b16 v[210:211], v241 offset:4352\n"
    "ds_read_b64_tr_b16 v[212:213], v241 offset:4608\n"
    "ds_read_b64_tr_b16 v[214:215], v241 offset:4864\n"
    "ds_read_b64_tr_b16 v[216:217], v241 offset:5120\n"
    "ds_read_b64_tr_b16 v[218:219], v241 offset:5376\n"
    "s_waitcnt lgkmcnt(12)\n"
    "v_mfma_f32_32x32x16_bf16 v[0:15], v[192:195], v[64:67], 0\n"
    "ds_read_b64_tr_b16 v[220:221], v241 offset:5632\n"
    "ds_read_b64_tr_b16 v[222:223], v241 offset:5888\n"
    "ds_write_b32 v226, v240 offset:32\n"
    "v_add_u32 v240, 1, v240\n"
    "s_waitcnt lgkmcnt(13)\n"
    "v_mfma_f32_32x32x16_bf16 v[16:31], v[196:199], v[64:67], 0\n"
    "ds_read_b64_tr_b16 v[192:193], v241 offset:8192\n"
    "ds_read_b64_tr_b16 v[194:195], v241 offset:8448\n"
    "s_waitcnt lgkmcnt(13)\n"
    "v_mfma_f32_32x32x16_bf16 v[32:47], v[200:203], v[64:67], 0\n"
    "ds_read_b64_tr_b16 v[196:197], v241 offset:8704\n"
    "ds_read_b64_tr_b16 v[198:199], v241 offset:8960\n"
    "s_waitcnt lgkmcnt(13)\n"
    "v_mfma_f32_32x32x16_bf16 v[48:63], v[204:207], v[64:67], 0\n"
    "ds_read_b64_tr_b16 v[200:201], v241 offset:9216\n"
    "ds_read_b64_tr_b16 v[202:203], v241 offset:9472\n"
    "s_waitcnt lgkmcnt(13)\n"
    "v_mfma_f32_32x32x16_bf16 v[0:15], v[208:211], v[68:71], v[0:15]\n"
    "ds_read_b64_tr_b16 v[204:205], v241 offset:9728\n"
    "ds_read_b64_tr_b16 v[206:207], v241 offset:9984\n"
    "s_waitcnt lgkmcnt(13)\n"
    "v_mfma_f32_32x32x16_bf16 v[16:31], v[212:215], v[68:71], v[16:31]\n"
    "ds_read_b64_tr_b16 v[208:209], v241 offset:12288\n"
    "ds_read_b64_tr_b16 v[210:211], v241 offset:12544\n"
    "ds_read_b32 v252, v225\n"
    "s_waitcnt lgkmcnt(14)\n"
    "v_mfma_f32_32x32x16_bf16 v[32:47], v[216:219], v[68:71], v[32:47]\n"
    "ds_read_b64_tr_b16 v[212:213], v241 offset:12800\n"
    "ds_read_b64_tr_b16 v[214:215], v241 offset:13056\n"
    "s_waitcnt lgkmcnt(14)\n"
    "v_mfma_f32_32x32x16_bf16 v[48:63], v[220:223], v[68:71], v[48:63]\n"
    "ds_read_b64_tr_b16 v[216:217], v241 offset:13312\n"
    "ds_read_b64_tr_b16 v[218:219], v241 offset:13568\n"
    "s_waitcnt lgkmcnt(13)\n"
    "v_mfma_f32_32x32x16_bf16 v[0:15], v[192:195], v[72:75], v[0:15]\n"
    "ds_read_b64_tr_b16 v[220:221], v241 offset:13824\n"
    "ds_read_b64_tr_b16 v[222:223], v241 offset:14080\n"
    "ds_write_b32 v226, v240 offset:32\n"
    "v_add_u32 v240, 1, v240\n"
    "s_waitcnt lgkmcnt(7)\n"
    "v_readfirstlane_b32 s72, v252\n"
    "s_mov_b32 s74, 4\n"
    "v_mfma_f32_32x32x16_bf16 v[16:31], v[196:199], v[72:75], v[16:31]\n"
    "s_cmp_ge_u32 s72, s74\n"
    "s_cbranch_scc0 .Lmha_QW1S\n"
    ".Lmha_QW1:\n"
    "ds_read_b64_tr_b16 v[192:193], v241 offset:16384\n"
    "ds_read_b64_tr_b16 v[194:195], v241 offset:16640\n"
    "v_mfma_f32_32x32x16_bf16 v[32:47], v[200:203], v[72:75], v[32:47]\n"
    "ds_read_b64_tr_b16 v[196:197], v241 offset:16896\n"
    "ds_read_b64_tr_b16 v[198:199], v241 offset:17152\n"
    "v_mfma_f32_32x32x16_bf16 v[48:63], v[204:207], v[72:75], v[48:63]\n"
    "ds_read_b64_tr_b16 v[200:201], v241 offset:17408\n"
    "ds_read_b64_tr_b16 v[202:203], v241 offset:17664\n"
    "v_mfma_f32_32x32x16_bf16 v[0:15], v[208:211], v[76:79], v[0:15]\n"
    "ds_read_b64_tr_b16 v[204:205], v241 offset:17920\n"
    "ds_read_b64_tr_b16 v[206:207], v241 offset:18176\n"
    "s_waitcnt vmcnt(4)\n"
    "ds_read_b128 v[80:83], v248 offset:4096\n"
    "ds_read_b128 v[84:87], v249 offset:4096\n"
    "ds_read_b128 v[88:91], v250 offset:4096\n"
    "ds_read_b128 v[92:95], v251 offset:4096\n"
    "s_waitcnt lgkmcnt(15)\n"
    "v_mfma_f32_32x32x16_bf16 v[16:31], v[212:215], v[76:79], v[16:31]\n"
    "ds_read_b64_tr_b16 v[208:209], v241 offset:20480\n"
    "ds_read_b64_tr_b16 v[210:211], v241 offset:20736\n"
    "v_mfma_f32_32x32x16_bf16 v[32:47], v[216:219], v[76:79], v[32:47]\n"
    "ds_read_b64_tr_b16 v[212:213], v241 offset:20992\n"
    "ds_read_b64_tr_b16 v[214:215], v241 offset:21248\n"
    "s_waitcnt lgkmcnt(15)\n"
    "v_mfma_f32_32x32x16_bf16 v[48:63], v[220:223], v[76:79], v[48:63]\n"
    "ds_read_b64_tr_b16 v[216:217], v241 offset:21504\n"
    "ds_read_b64_tr_b16 v[218:219], v241 offset:21760\n"
    "s_waitcnt lgkmcnt(6)\n"
    "v_mfma_f32_32x32x16_bf16 v[0:15], v[192:195], v[80:83], v[0:15]\n"
    "ds_read_b64_tr_b16 v[220:221], v241 offset:22016\n"
    "ds_read_b64_tr_b16 v[222:223], v241 offset:22272\n"
    "ds_write_b32 v226, v240 offset:32\n"
    "v_add_u32 v240, 1, v240\n"
    "s_add_u32 m0, s76, 3712\n"
    "s_nop 0\n"
    "buffer_load_dwordx4 v246, s[40:43], 0 offen offset:384 lds\n"
    "s_add_u32 m0, s76, 4736\n"
    "s_nop 0\n"
    "buffer_load_dwordx4 v247, s[40:43], 0 offen offset:384 lds\n"
    "v_mfma_f32_32x32x16_bf16 v[16:31], v[196:199], v[80:83], v[16:31]\n"
    "ds_read_b64_tr_b16 v[192:193], v241 offset:24576\n"
    "ds_read_b64_tr_b16 v[194:195], v241 offset:24832\n"
    "s_add_u32 m0, s76, 5760\n"
    "s_nop 0\n"
    "buffer_load_dwordx4 v233, s[40:43], 0 offen offset:384 lds\n"
    "s_add_u32 m0, s76, 6784\n"
    "s_nop 0\n"
    "buffer_load_dwordx4 v234, s[40:43], 0 offen offset:384 lds\n"
    "v_mfma_f32_32x32x16_bf16 v[32:47], v[200:203], v[80:83], v[32:47]\n"
    "ds_read_b64_tr_b16 v[196:197], v241 offset:25088\n"
    "ds_read_b64_tr_b16 v[198:199], v241 offset:25344\n"
    "v_mfma_f32_32x32x16_bf16 v[48:63], v[204:207], v[80:83], v[48:63]\n"
    "ds_read_b64_tr_b16 v[200:201], v241 offset:25600\n"
    "ds_read_b64_tr_b16 v[202:203], v241 offset:25856\n"
    "s_waitcnt lgkmcnt(13)\n"
    "v_mfma_f32_32x32x16_bf16 v[0:15], v[208:211], v[84:87], v[0:15]\n"
    "ds_read_b64_tr_b16 v[204:205], v241 offset:26112\n"
    "ds_read_b64_tr_b16 v[206:207], v241 offset:26368\n"
    "s_waitcnt lgkmcnt(13)\n"
    "v_mfma_f32_32x32x16_bf16 v[16:31], v[212:215], v[84:87], v[16:31]\n"
    "ds_read_b64_tr_b16 v[208:209], v241 offset:28672\n"
    "ds_read_b64_tr_b16 v[210:211], v241 offset:28928\n"
    "ds_read_b32 v252, v225\n"
    "s_waitcnt lgkmcnt(14)\n"
    "v_mfma_f32_32x32x16_bf16 v[32:47], v[216:219], v[84:87], v[32:47]\n"
    "ds_read_b64_tr_b16 v[212:213], v241 offset:29184\n"
    "ds_read_b64_tr_b16 v[214:215], v241 offset:29440\n"
    "s_waitcnt lgkmcnt(14)\n"
    "v_mfma_f32_32x32x16_bf16 v[48:63], v[220:223], v[84:87], v[48:63]\n"
    "ds_read_b64_tr_b16 v[216:217], v241 offset:29696\n"
    "ds_read_b64_tr_b16 v[218:219], v241 offset:29952\n"
    "s_waitcnt lgkmcnt(13)\n"
    "v_mfma_f32_32x32x16_bf16 v[0:15], v[192:195], v[88:91], v[0:15]\n"
    "ds_read_b64_tr_b16 v[220:221], v241 offset:30208\n"
    "ds_read_b64_tr_b16 v[222:223], v241 offset:30464\n"
    "ds_write_b32 v226, v240 offset:32\n"
    "v_add_u32 v240, 1, v240\n"
    "s_waitcnt lgkmcnt(7)\n"
    "v_readfirstlane_b32 s72, v252\n"
    "s_mov_b32 s74, 6\n"
    "v_mfma_f32_32x32x16_bf16 v[16:31], v[196:199], v[88:91], v[16:31]\n"
    "s_cmp_ge_u32 s72, s74\n"
    "s_cbranch_scc0 .Lmha_QW2S\n"
    ".Lmha_QW2:\n"
    "ds_read_b64_tr_b16 v[192:193], v241 offset:32768\n"
    "ds_read_b64_tr_b16 v[194:195], v241 offset:33024\n"
    "v_mfma_f32_32x32x16_bf16 v[32:47], v[200:203], v[88:91], v[32:47]\n"
    "ds_read_b64_tr_b16 v[196:197], v241 offset:33280\n"
    "ds_read_b64_tr_b16 v[198:199], v241 offset:33536\n"
    "v_mfma_f32_32x32x16_bf16 v[48:63], v[204:207], v[88:91], v[48:63]\n"
    "ds_read_b64_tr_b16 v[200:201], v241 offset:33792\n"
    "ds_read_b64_tr_b16 v[202:203], v241 offset:34048\n"
    "v_mfma_f32_32x32x16_bf16 v[0:15], v[208:211], v[92:95], v[0:15]\n"
    "ds_read_b64_tr_b16 v[204:205], v241 offset:34304\n"
    "ds_read_b64_tr_b16 v[206:207], v241 offset:34560\n"
    "s_waitcnt vmcnt(4)\n"
    "ds_read_b128 v[96:99], v248 offset:0\n"
    "ds_read_b128 v[100:103], v249 offset:0\n"
    "ds_read_b128 v[104:107], v250 offset:0\n"
    "ds_read_b128 v[108:111], v251 offset:0\n"
    "s_waitcnt lgkmcnt(15)\n"
    "v_mfma_f32_32x32x16_bf16 v[16:31], v[212:215], v[92:95], v[16:31]\n"
    "ds_read_b64_tr_b16 v[208:209], v241 offset:36864\n"
    "ds_read_b64_tr_b16 v[210:211], v241 offset:37120\n"
    "v_mfma_f32_32x32x16_bf16 v[32:47], v[216:219], v[92:95], v[32:47]\n"
    "ds_read_b64_tr_b16 v[212:213], v241 offset:37376\n"
    "ds_read_b64_tr_b16 v[214:215], v241 offset:37632\n"
    "s_waitcnt lgkmcnt(15)\n"
    "v_mfma_f32_32x32x16_bf16 v[48:63], v[220:223], v[92:95], v[48:63]\n"
    "ds_read_b64_tr_b16 v[216:217], v241 offset:37888\n"
    "ds_read_b64_tr_b16 v[218:219], v241 offset:38144\n"
    "s_waitcnt lgkmcnt(6)\n"
    "v_mfma_f32_32x32x16_bf16 v[0:15], v[192:195], v[96:99], v[0:15]\n"
    "ds_read_b64_tr_b16 v[220:221], v241 offset:38400\n"
    "ds_read_b64_tr_b16 v[222:223], v241 offset:38656\n"
    "ds_write_b32 v226, v240 offset:32\n"
    "v_add_u32 v240, 1, v240\n"
    "s_add_u32 m0, s76, 4294966784\n"
    "s_nop 0\n"
    "buffer_load_dwordx4 v246, s[40:43], 0 offen offset:512 lds\n"
    "s_add_u32 m0, s76, 512\n"
    "s_nop 0\n"
    "buffer_load_dwordx4 v247, s[40:43], 0 offen offset:512 lds\n"
    "v_mfma_f32_32x32x16_bf16 v[16:31], v[196:199], v[96:99], v[16:31]\n"
    "ds_read_b64_tr_b16 v[192:193], v241 offset:40960\n"
    "ds_read_b64_tr_b16 v[194:195], v241 offset:41216\n"
    "s_add_u32 m0, s76, 1536\n"
    "s_nop 0\n"
    "buffer_load_dwordx4 v233, s[40:43], 0 offen offset:512 lds\n"
    "s_add_u32 m0, s76, 2560\n"
    "s_nop 0\n"
    "buffer_load_dwordx4 v234, s[40:43], 0 offen offset:512 lds\n"
    "v_mfma_f32_32x32x16_bf16 v[32:47], v[200:203], v[96:99], v[32:47]\n"
    "ds_read_b64_tr_b16 v[196:197], v241 offset:41472\n"
    "ds_read_b64_tr_b16 v[198:199], v241 offset:41728\n"
    "v_mfma_f32_32x32x16_bf16 v[48:63], v[204:207], v[96:99], v[48:63]\n"
    "ds_read_b64_tr_b16 v[200:201], v241 offset:41984\n"
    "ds_read_b64_tr_b16 v[202:203], v241 offset:42240\n"
    "s_waitcnt lgkmcnt(13)\n"
    "v_mfma_f32_32x32x16_bf16 v[0:15], v[208:211], v[100:103], v[0:15]\n"
    "ds_read_b64_tr_b16 v[204:205], v241 offset:42496\n"
    "ds_read_b64_tr_b16 v[206:207], v241 offset:42752\n"
    "s_waitcnt lgkmcnt(13)\n"
    "v_mfma_f32_32x32x16_bf16 v[16:31], v[212:215], v[100:103], v[16:31]\n"
    "ds_read_b64_tr_b16 v[208:209], v241 offset:45056\n"
    "ds_read_b64_tr_b16 v[210:211], v241 offset:45312\n"
    "ds_read_b32 v252, v225\n"
    "s_waitcnt lgkmcnt(14)\n"
    "v_mfma_f32_32x32x16_bf16 v[32:47], v[216:219], v[100:103], v[32:47]\n"
    "ds_read_b64_tr_b16 v[212:213], v241 offset:45568\n"
    "ds_read_b64_tr_b16 v[214:215], v241 offset:45824\n"
    "s_waitcnt lgkmcnt(14)\n"
    "v_mfma_f32_32x32x16_bf16 v[48:63], v[220:223], v[100:103], v[48:63]\n"
    "ds_read_b64_tr_b16 v[216:217], v241 offset:46080\n"
    "ds_read_b64_tr_b16 v[218:219], v241 offset:46336\n"
    "s_waitcnt lgkmcnt(13)\n"
    "v_mfma_f32_32x32x16_bf16 v[0:15], v[192:195], v[104:107], v[0:15]\n"
    "ds_read_b64_tr_b16 v[220:221], v241 offset:46592\n"
    "ds_read_b64_tr_b16 v[222:223], v241 offset:46848\n"
    "ds_write_b32 v226, v240 offset:32\n"
    "v_add_u32 v240, 1, v240\n"
    "s_waitcnt lgkmcnt(7)\n"
    "v_readfirstlane_b32 s72, v252\n"
    "s_mov_b32 s74, 8\n"
    "v_mfma_f32_32x32x16_bf16 v[16:31], v[196:199], v[104:107], v[16:31]\n"
    "s_cmp_ge_u32 s72, s74\n"
    "s_cbranch_scc0 .Lmha_QW3S\n"
    ".Lmha_QW3:\n"
    "ds_read_b64_tr_b16 v[192:193], v241 offset:49152\n"
    "ds_read_b64_tr_b16 v[194:195], v241 offset:49408\n"
    "v_mfma_f32_32x32x16_bf16 v[32:47], v[200:203], v[104:107], v[32:47]\n"
    "ds_read_b64_tr_b16 v[196:197], v241 offset:49664\n"
    "ds_read_b64_tr_b16 v[198:199], v241 offset:49920\n"
    "v_mfma_f32_32x32x16_bf16 v[48:63], v[204:207], v[104:107], v[48:63]\n"
    "ds_read_b64_tr_b16 v[200:201], v241 offset:50176\n"
    "ds_read_b64_tr_b16 v[202:203], v241 offset:50432\n"
    "v_mfma_f32_32x32x16_bf16 v[0:15], v[208:211], v[108:111], v[0:15]\n"
    "ds_read_b64_tr_b16 v[204:205], v241 offset:50688\n"
    "ds_read_b64_tr_b16 v[206:207], v241 offset:50944\n"
    "s_waitcnt vmcnt(4)\n"
    "ds_read_b128 v[112:115], v248 offset:4096\n"
    "ds_read_b128 v[116:119], v249 offset:4096\n"
    "ds_read_b128 v[120:123], v250 offset:4096\n"
    "ds_read_b128 v[124:127], v251 offset:4096\n"
    "s_waitcnt lgkmcnt(15)\n"
    "v_mfma_f32_32x32x16_bf16 v[16:31], v[212:215], v[108:111], v[16:31]\n"
    "ds_read_b64_tr_b16 v[208:209], v241 offset:53248\n"
    "ds_read_b64_tr_b16 v[210:211], v241 offset:53504\n"
    "v_mfma_f32_32x32x16_bf16 v[32:47], v[216:219], v[108:111], v[32:47]\n"
    "ds_read_b64_tr_b16 v[212:213], v241 offset:53760\n"
    "ds_read_b64_tr_b16 v[214:215], v241 offset:54016\n"
    "s_waitcnt lgkmcnt(15)\n"
    "v_mfma_f32_32x32x16_bf16 v[48:63], v[220:223], v[108:111], v[48:63]\n"
    "ds_read_b64_tr_b16 v[216:217], v241 offset:54272\n"
    "ds_read_b64_tr_b16 v[218:219], v241 offset:54528\n"
    "s_waitcnt lgkmcnt(6)\n"
    "v_mfma_f32_32x32x16_bf16 v[0:15], v[192:195], v[112:115], v[0:15]\n"
    "ds_read_b64_tr_b16 v[220:221], v241 offset:54784\n"
    "ds_read_b64_tr_b16 v[222:223], v241 offset:55040\n"
    "ds_write_b32 v226, v240 offset:32\n"
    "v_add_u32 v240, 1, v240\n"
    "s_add_u32 m0, s76, 3456\n"
    "s_nop 0\n"
    "buffer_load_dwordx4 v246, s[40:43], 0 offen offset:640 lds\n"
    "s_add_u32 m0, s76, 4480\n"
    "s_nop 0\n"
    "buffer_load_dwordx4 v247, s[40:43], 0 offen offset:640 lds\n"
    "v_mfma_f32_32x32x16_bf16 v[16:31], v[196:199], v[112:115], v[16:31]\n"
    "ds_read_b64_tr_b16 v[192:193], v241 offset:57344\n"
    "ds_read_b64_tr_b16 v[194:195], v241 offset:57600\n"
    "s_add_u32 m0, s76, 5504\n"
    "s_nop 0\n"
    "buffer_load_dwordx4 v233, s[40:43], 0 offen offset:640 lds\n"
    "s_add_u32 m0, s76, 6528\n"
    "s_nop 0\n"
    "buffer_load_dwordx4 v234, s[40:43], 0 offen offset:640 lds\n"
    "v_mfma_f32_32x32x16_bf16 v[32:47], v[200:203], v[112:115], v[32:47]\n"
    "ds_read_b64_tr_b16 v[196:197], v241 offset:57856\n"
    "ds_read_b64_tr_b16 v[198:199], v241 offset:58112\n"
    "v_mfma_f32_32x32x16_bf16 v[48:63], v[204:207], v[112:115], v[48:63]\n"
    "ds_read_b64_tr_b16 v[200:201], v241 offset:58368\n"
    "ds_read_b64_tr_b16 v[202:203], v241 offset:58624\n"
    "s_waitcnt lgkmcnt(13)\n"
    "v_mfma_f32_32x32x16_bf16 v[0:15], v[208:211], v[116:119], v[0:15]\n"
    "ds_read_b64_tr_b16 v[204:205], v241 offset:58880\n"
    "ds_read_b64_tr_b16 v[206:207], v241 offset:59136\n"
    "s_waitcnt lgkmcnt(13)\n"
    "v_mfma_f32_32x32x16_bf16 v[16:31], v[212:215], v[116:119], v[16:31]\n"
    "ds_read_b64_tr_b16 v[208:209], v241 offset:61440\n"
    "ds_read_b64_tr_b16 v[210:211], v241 offset:61696\n"
    "ds_read_b32 v252, v225\n"
    "s_waitcnt lgkmcnt(14)\n"
    "v_mfma_f32_32x32x16_bf16 v[32:47], v[216:219], v[116:119], v[32:47]\n"
    "ds_read_b64_tr_b16 v[212:213], v241 offset:61952\n"
    "ds_read_b64_tr_b16 v[214:215], v241 offset:62208\n"
    "s_waitcnt lgkmcnt(14)\n"
    "v_mfma_f32_32x32x16_bf16 v[48:63], v[220:223], v[116:119], v[48:63]\n"
    "ds_read_b64_tr_b16 v[216:217], v241 offset:62464\n"
    "ds_read_b64_tr_b16 v[218:219], v241 offset:62720\n"
    "s_waitcnt lgkmcnt(13)\n"
    "v_mfma_f32_32x32x16_bf16 v[0:15], v[192:195], v[120:123], v[0:15]\n"
    "ds_read_b64_tr_b16 v[220:221], v241 offset:62976\n"
    "ds_read_b64_tr_b16 v[222:223], v241 offset:63232\n"
    "ds_write_b32 v226, v240 offset:32\n"
    "v_add_u32 v240, 1, v240\n"
    "s_waitcnt lgkmcnt(7)\n"
    "v_readfirstlane_b32 s72, v252\n"
    "s_mov_b32 s74, 10\n"
    "v_mfma_f32_32x32x16_bf16 v[16:31], v[196:199], v[120:123], v[16:31]\n"
    "s_cmp_ge_u32 s72, s74\n"
    "s_cbranch_scc0 .Lmha_QW4S\n"
    ".Lmha_QW4:\n"
    "ds_read_b64_tr_b16 v[192:193], v232 offset:0\n"
    "ds_read_b64_tr_b16 v[194:195], v232 offset:256\n"
    "v_mfma_f32_32x32x16_bf16 v[32:47], v[200:203], v[120:123], v[32:47]\n"
    "ds_read_b64_tr_b16 v[196:197], v232 offset:512\n"
    "ds_read_b64_tr_b16 v[198:199], v232 offset:768\n"
    "v_mfma_f32_32x32x16_bf16 v[48:63], v[204:207], v[120:123], v[48:63]\n"
    "ds_read_b64_tr_b16 v[200:201], v232 offset:1024\n"
    "ds_read_b64_tr_b16 v[202:203], v232 offset:1280\n"
    "v_mfma_f32_32x32x16_bf16 v[0:15], v[208:211], v[124:127], v[0:15]\n"
    "ds_read_b64_tr_b16 v[204:205], v232 offset:1536\n"
    "ds_read_b64_tr_b16 v[206:207], v232 offset:1792\n"
    "s_waitcnt vmcnt(4)\n"
    "ds_read_b128 v[128:131], v248 offset:0\n"
    "ds_read_b128 v[132:135], v249 offset:0\n"
    "ds_read_b128 v[136:139], v250 offset:0\n"
    "ds_read_b128 v[140:143], v251 offset:0\n"
    "s_waitcnt lgkmcnt(15)\n"
    "v_mfma_f32_32x32x16_bf16 v[16:31], v[212:215], v[124:127], v[16:31]\n"
    "ds_read_b64_tr_b16 v[208:209], v232 offset:4096\n"
    "ds_read_b64_tr_b16 v[210:211], v232 offset:4352\n"
    "v_mfma_f32_32x32x16_bf16 v[32:47], v[216:219], v[124:127], v[32:47]\n"
    "ds_read_b64_tr_b16 v[212:213], v232 offset:4608\n"
    "ds_read_b64_tr_b16 v[214:215], v232 offset:4864\n"
    "s_waitcnt lgkmcnt(15)\n"
    "v_mfma_f32_32x32x16_bf16 v[48:63], v[220:223], v[124:127], v[48:63]\n"
    "ds_read_b64_tr_b16 v[216:217], v232 offset:5120\n"
    "ds_read_b64_tr_b16 v[218:219], v232 offset:5376\n"
    "s_waitcnt lgkmcnt(6)\n"
    "v_mfma_f32_32x32x16_bf16 v[0:15], v[192:195], v[128:131], v[0:15]\n"
    "ds_read_b64_tr_b16 v[220:221], v232 offset:5632\n"
    "ds_read_b64_tr_b16 v[222:223], v232 offset:5888\n"
    "ds_write_b32 v226, v240 offset:32\n"
    "v_add_u32 v240, 1, v240\n"
    "s_add_u32 m0, s76, 4294966528\n"
    "s_nop 0\n"
    "buffer_load_dwordx4 v246, s[40:43], 0 offen offset:768 lds\n"
    "s_add_u32 m0, s76, 256\n"
    "s_nop 0\n"
    "buffer_load_dwordx4 v247, s[40:43], 0 offen offset:768 lds\n"
    "v_mfma_f32_32x32x16_bf16 v[16:31], v[196:199], v[128:131], v[16:31]\n"
    "ds_read_b64_tr_b16 v[192:193], v232 offset:8192\n"
    "ds_read_b64_tr_b16 v[194:195], v232 offset:8448\n"
    "s_add_u32 m0, s76, 1280\n"
    "s_nop 0\n"
    "buffer_load_dwordx4 v233, s[40:43], 0 offen offset:768 lds\n"
    "s_add_u32 m0, s76, 2304\n"
    "s_nop 0\n"
    "buffer_load_dwordx4 v234, s[40:43], 0 offen offset:768 lds\n"
    "v_mfma_f32_32x32x16_bf16 v[32:47], v[200:203], v[128:131], v[32:47]\n"
    "ds_read_b64_tr_b16 v[196:197], v232 offset:8704\n"
    "ds_read_b64_tr_b16 v[198:199], v232 offset:8960\n"
    "v_mfma_f32_32x32x16_bf16 v[48:63], v[204:207], v[128:131], v[48:63]\n"
    "ds_read_b64_tr_b16 v[200:201], v232 offset:9216\n"
    "ds_read_b64_tr_b16 v[202:203], v232 offset:9472\n"
    "s_waitcnt lgkmcnt(13)\n"
    "v_mfma_f32_32x32x16_bf16 v[0:15], v[208:211], v[132:135], v[0:15]\n"
    "ds_read_b64_tr_b16 v[204:205], v232 offset:9728\n"
    "ds_read_b64_tr_b16 v[206:207], v232 offset:9984\n"
    "s_waitcnt lgkmcnt(13)\n"
    "v_mfma_f32_32x32x16_bf16 v[16:31], v[212:215], v[132:135], v[16:31]\n"
    "ds_read_b64_tr_b16 v[208:209], v232 offset:12288\n"
    "ds_read_b64_tr_b16 v[210:211], v232 offset:12544\n"
    "ds_read_b32 v252, v225\n"
    "s_waitcnt lgkmcnt(14)\n"
    "v_mfma_f32_32x32x16_bf16 v[32:47], v[216:219], v[132:135], v[32:47]\n"
    "ds_read_b64_tr_b16 v[212:213], v232 offset:12800\n"
    "ds_read_b64_tr_b16 v[214:215], v232 offset:13056\n"
    "s_waitcnt lgkmcnt(14)\n"
    "v_mfma_f32_32x32x16_bf16 v[48:63], v[220:223], v[132:135], v[48:63]\n"
    "ds_read_b64_tr_b16 v[216:217], v232 offset:13312\n"
    "ds_read_b64_tr_b16 v[218:219], v232 offset:13568\n"
    "s_waitcnt lgkmcnt(13)\n"
    "v_mfma_f32_32x32x16_bf16 v[0:15], v[192:195], v[136:139], v[0:15]\n"
    "ds_read_b64_tr_b16 v[220:221], v232 offset:13824\n"
    "ds_read_b64_tr_b16 v[222:223], v232 offset:14080\n"
    "ds_write_b32 v226, v240 offset:32\n"
    "v_add_u32 v240, 1, v240\n"
    "s_waitcnt lgkmcnt(7)\n"
    "v_readfirstlane_b32 s72, v252\n"
    "s_mov_b32 s74, 12\n"
    "v_mfma_f32_32x32x16_bf16 v[16:31], v[196:199], v[136:139], v[16:31]\n"
    "s_cmp_ge_u32 s72, s74\n"
    "s_cbranch_scc0 .Lmha_QW5S\n"
    ".Lmha_QW5:\n"
    "ds_read_b64_tr_b16 v[192:193], v232 offset:16384\n"
    "ds_read_b64_tr_b16 v[194:195], v232 offset:16640\n"
    "v_mfma_f32_32x32x16_bf16 v[32:47], v[200:203], v[136:139], v[32:47]\n"
    "ds_read_b64_tr_b16 v[196:197], v232 offset:16896\n"
    "ds_read_b64_tr_b16 v[198:199], v232 offset:17152\n"
    "v_mfma_f32_32x32x16_bf16 v[48:63], v[204:207], v[136:139], v[48:63]\n"
    "ds_read_b64_tr_b16 v[200:201], v232 offset:17408\n"
    "ds_read_b64_tr_b16 v[202:203], v232 offset:17664\n"
    "v_mfma_f32_32x32x16_bf16 v[0:15], v[208:211], v[140:143], v[0:15]\n"
    "ds_read_b64_tr_b16 v[204:205], v232 offset:17920\n"
    "ds_read_b64_tr_b16 v[206:207], v232 offset:18176\n"
    "s_waitcnt vmcnt(4)\n"
    "ds_read_b128 v[144:147], v248 offset:4096\n"
    "ds_read_b128 v[148:151], v249 offset:4096\n"
    "ds_read_b128 v[152:155], v250 offset:4096\n"
    "ds_read_b128 v[156:159], v251 offset:4096\n"
    "s_waitcnt lgkmcnt(15)\n"
    "v_mfma_f32_32x32x16_bf16 v[16:31], v[212:215], v[140:143], v[16:31]\n"
    "ds_read_b64_tr_b16 v[208:209], v232 offset:20480\n"
    "ds_read_b64_tr_b16 v[210:211], v232 offset:20736\n"
    "v_mfma_f32_32x32x16_bf16 v[32:47], v[216:219], v[140:143], v[32:47]\n"
    "ds_read_b64_tr_b16 v[212:213], v232 offset:20992\n"
    "ds_read_b64_tr_b16 v[214:215], v232 offset:21248\n"
    "s_waitcnt lgkmcnt(15)\n"
    "v_mfma_f32_32x32x16_bf16 v[48:63], v[220:223], v[140:143], v[48:63]\n"
    "ds_read_b64_tr_b16 v[216:217], v232 offset:21504\n"
    "ds_read_b64_tr_b16 v[218:219], v232 offset:21760\n"
    "s_waitcnt lgkmcnt(6)\n"
    "v_mfma_f32_32x32x16_bf16 v[0:15], v[192:195], v[144:147], v[0:15]\n"
    "ds_read_b64_tr_b16 v[220:221], v232 offset:22016\n"
    "ds_read_b64_tr_b16 v[222:223], v232 offset:22272\n"
    "ds_write_b32 v226, v240 offset:32\n"
    "v_add_u32 v240, 1, v240\n"
    "s_add_u32 m0, s76, 3200\n"
    "s_nop 0\n"
    "buffer_load_dwordx4 v246, s[40:43], 0 offen offset:896 lds\n"
    "s_add_u32 m0, s76, 4224\n"
    "s_nop 0\n"
    "buffer_load_dwordx4 v247, s[40:43], 0 offen offset:896 lds\n"
    "v_mfma_f32_32x32x16_bf16 v[16:31], v[196:199], v[144:147], v[16:31]\n"
    "ds_read_b64_tr_b16 v[192:193], v232 offset:24576\n"
    "ds_read_b64_tr_b16 v[194:195], v232 offset:24832\n"
    "s_add_u32 m0, s76, 5248\n"
    "s_nop 0\n"
    "buffer_load_dwordx4 v233, s[40:43], 0 offen offset:896 lds\n"
    "s_add_u32 m0, s76, 6272\n"
    "s_nop 0\n"
    "buffer_load_dwordx4 v234, s[40:43], 0 offen offset:896 lds\n"
    "v_mfma_f32_32x32x16_bf16 v[32:47], v[200:203], v[144:147], v[32:47]\n"
    "ds_read_b64_tr_b16 v[196:197], v232 offset:25088\n"
    "ds_read_b64_tr_b16 v[198:199], v232 offset:25344\n"
    "v_mfma_f32_32x32x16_bf16 v[48:63], v[204:207], v[144:147], v[48:63]\n"
    "ds_read_b64_tr_b16 v[200:201], v232 offset:25600\n"
    "ds_read_b64_tr_b16 v[202:203], v232 offset:25856\n"
    "s_waitcnt lgkmcnt(13)\n"
    "v_mfma_f32_32x32x16_bf16 v[0:15], v[208:211], v[148:151], v[0:15]\n"
    "ds_read_b64_tr_b16 v[204:205], v232 offset:26112\n"
    "ds_read_b64_tr_b16 v[206:207], v232 offset:26368\n"
    "s_waitcnt lgkmcnt(13)\n"
    "v_mfma_f32_32x32x16_bf16 v[16:31], v[212:215], v[148:151], v[16:31]\n"
    "ds_read_b64_tr_b16 v[208:209], v232 offset:28672\n"
    "ds_read_b64_tr_b16 v[210:211], v232 offset:28928\n"
    "ds_read_b32 v252, v225\n"
    "s_waitcnt lgkmcnt(14)\n"
    "v_mfma_f32_32x32x16_bf16 v[32:47], v[216:219], v[148:151], v[32:47]\n"
    "ds_read_b64_tr_b16 v[212:213], v232 offset:29184\n"
    "ds_read_b64_tr_b16 v[214:215], v232 offset:29440\n"
    "s_waitcnt lgkmcnt(14)\n"
    "v_mfma_f32_32x32x16_bf16 v[48:63], v[220:223], v[148:151], v[48:63]\n"
    "ds_read_b64_tr_b16 v[216:217], v232 offset:29696\n"
    "ds_read_b64_tr_b16 v[218:219], v232 offset:29952\n"
    "s_waitcnt lgkmcnt(13)\n"
    "v_mfma_f32_32x32x16_bf16 v[0:15], v[192:195], v[152:155], v[0:15]\n"
    "ds_read_b64_tr_b16 v[220:221], v232 offset:30208\n"
    "ds_read_b64_tr_b16 v[222:223], v232 offset:30464\n"
    "ds_write_b32 v226, v240 offset:32\n"
    "v_add_u32 v240, 1, v240\n"
    "s_waitcnt lgkmcnt(7)\n"
    "v_readfirstlane_b32 s72, v252\n"
    "s_mov_b32 s74, 14\n"
    "v_mfma_f32_32x32x16_bf16 v[16:31], v[196:199], v[152:155], v[16:31]\n"
    "s_cmp_ge_u32 s72, s74\n"
    "s_cbranch_scc0 .Lmha_QW6S\n"
    ".Lmha_QW6:\n"
    "ds_read_b64_tr_b16 v[192:193], v232 offset:32768\n"
    "ds_read_b64_tr_b16 v[194:195], v232 offset:33024\n"
    "v_mfma_f32_32x32x16_bf16 v[32:47], v[200:203], v[152:155], v[32:47]\n"
    "ds_read_b64_tr_b16 v[196:197], v232 offset:33280\n"
    "ds_read_b64_tr_b16 v[198:199], v232 offset:33536\n"
    "v_mfma_f32_32x32x16_bf16 v[48:63], v[204:207], v[152:155], v[48:63]\n"
    "ds_read_b64_tr_b16 v[200:201], v232 offset:33792\n"
    "ds_read_b64_tr_b16 v[202:203], v232 offset:34048\n"
    "v_mfma_f32_32x32x16_bf16 v[0:15], v[208:211], v[156:159], v[0:15]\n"
    "ds_read_b64_tr_b16 v[204:205], v232 offset:34304\n"
    "ds_read_b64_tr_b16 v[206:207], v232 offset:34560\n"
    "s_waitcnt vmcnt(4)\n"
    "ds_read_b128 v[160:163], v248 offset:0\n"
    "ds_read_b128 v[164:167], v249 offset:0\n"
    "ds_read_b128 v[168:171], v250 offset:0\n"
    "ds_read_b128 v[172:175], v251 offset:0\n"
    "s_waitcnt lgkmcnt(15)\n"
    "v_mfma_f32_32x32x16_bf16 v[16:31], v[212:215], v[156:159], v[16:31]\n"
    "ds_read_b64_tr_b16 v[208:209], v232 offset:36864\n"
    "ds_read_b64_tr_b16 v[210:211], v232 offset:37120\n"
    "v_mfma_f32_32x32x16_bf16 v[32:47], v[216:219], v[156:159], v[32:47]\n"
    "ds_read_b64_tr_b16 v[212:213], v232 offset:37376\n"
    "ds_read_b64_tr_b16 v[214:215], v232 offset:37632\n"
    "s_waitcnt lgkmcnt(15)\n"
    "v_mfma_f32_32x32x16_bf16 v[48:63], v[220:223], v[156:159], v[48:63]\n"
    "ds_read_b64_tr_b16 v[216:217], v232 offset:37888\n"
    "ds_read_b64_tr_b16 v[218:219], v232 offset:38144\n"
    "s_waitcnt lgkmcnt(9)\n"
    "v_mfma_f32_32x32x16_bf16 v[0:15], v[192:195], v[160:163], v[0:15]\n"
    "ds_read_b64_tr_b16 v[220:221], v232 offset:38400\n"
    "ds_read_b64_tr_b16 v[222:223], v232 offset:38656\n"
    "ds_write_b32 v226, v240 offset:32\n"
    "v_add_u32 v240, 1, v240\n"
    "v_mfma_f32_32x32x16_bf16 v[16:31], v[196:199], v[160:163], v[16:31]\n"
    "ds_read_b64_tr_b16 v[192:193], v232 offset:40960\n"
    "ds_read_b64_tr_b16 v[194:195], v232 offset:41216\n"
    "v_mfma_f32_32x32x16_bf16 v[32:47], v[200:203], v[160:163], v[32:47]\n"
    "ds_read_b64_tr_b16 v[196:197], v232 offset:41472\n"
    "ds_read_b64_tr_b16 v[198:199], v232 offset:41728\n"
    "v_mfma_f32_32x32x16_bf16 v[48:63], v[204:207], v[160:163], v[48:63]\n"
    "ds_read_b64_tr_b16 v[200:201], v232 offset:41984\n"
    "ds_read_b64_tr_b16 v[202:203], v232 offset:42240\n"
    "s_waitcnt lgkmcnt(13)\n"
    "v_mfma_f32_32x32x16_bf16 v[0:15], v[208:211], v[164:167], v[0:15]\n"
    "ds_read_b64_tr_b16 v[204:205], v232 offset:42496\n"
    "ds_read_b64_tr_b16 v[206:207], v232 offset:42752\n"
    "s_waitcnt lgkmcnt(13)\n"
    "v_mfma_f32_32x32x16_bf16 v[16:31], v[212:215], v[164:167], v[16:31]\n"
    "ds_read_b64_tr_b16 v[208:209], v232 offset:45056\n"
    "ds_read_b64_tr_b16 v[210:211], v232 offset:45312\n"
    "ds_read_b32 v252, v225\n"
    "s_waitcnt lgkmcnt(14)\n"
    "v_mfma_f32_32x32x16_bf16 v[32:47], v[216:219], v[164:167], v[32:47]\n"
    "ds_read_b64_tr_b16 v[212:213], v232 offset:45568\n"
    "ds_read_b64_tr_b16 v[214:215], v232 offset:45824\n"
    "s_waitcnt lgkmcnt(14)\n"
    "v_mfma_f32_32x32x16_bf16 v[48:63], v[220:223], v[164:167], v[48:63]\n"
    "ds_read_b64_tr_b16 v[216:217], v232 offset:46080\n"
    "ds_read_b64_tr_b16 v[218:219], v232 offset:46336\n"
    "s_waitcnt lgkmcnt(13)\n"
    "v_mfma_f32_32x32x16_bf16 v[0:15], v[192:195], v[168:171], v[0:15]\n"
    "ds_read_b64_tr_b16 v[220:221], v232 offset:46592\n"
    "ds_read_b64_tr_b16 v[222:223], v232 offset:46848\n"
    "ds_write_b32 v226, v240 offset:32\n"
    "v_add_u32 v240, 1, v240\n"
    "s_waitcnt lgkmcnt(7)\n"
    "v_readfirstlane_b32 s72, v252\n"
    "s_mov_b32 s74, 16\n"
    "v_mfma_f32_32x32x16_bf16 v[16:31], v[196:199], v[168:171], v[16:31]\n"
    "s_cmp_ge_u32 s72, s74\n"
    "s_cbranch_scc0 .Lmha_QW7S\n"
    ".Lmha_QW7:\n"
    "ds_read_b64_tr_b16 v[192:193], v232 offset:49152\n"
    "ds_read_b64_tr_b16 v[194:195], v232 offset:49408\n"
    "v_mfma_f32_32x32x16_bf16 v[32:47], v[200:203], v[168:171], v[32:47]\n"
    "ds_read_b64_tr_b16 v[196:197], v232 offset:49664\n"
    "ds_read_b64_tr_b16 v[198:199], v232 offset:49920\n"
    "v_mfma_f32_32x32x16_bf16 v[48:63], v[204:207], v[168:171], v[48:63]\n"
    "ds_read_b64_tr_b16 v[200:201], v232 offset:50176\n"
    "ds_read_b64_tr_b16 v[202:203], v232 offset:50432\n"
    "v_mfma_f32_32x32x16_bf16 v[0:15], v[208:211], v[172:175], v[0:15]\n"
    "ds_read_b64_tr_b16 v[204:205], v232 offset:50688\n"
    "ds_read_b64_tr_b16 v[206:207], v232 offset:50944\n"
    "s_waitcnt vmcnt(0)\n"
    "ds_read_b128 v[176:179], v248 offset:4096\n"
    "ds_read_b128 v[180:183], v249 offset:4096\n"
    "ds_read_b128 v[184:187], v250 offset:4096\n"
    "ds_read_b128 v[188:191], v251 offset:4096\n"
    "s_waitcnt lgkmcnt(15)\n"
    "v_mfma_f32_32x32x16_bf16 v[16:31], v[212:215], v[172:175], v[16:31]\n"
    "ds_read_b64_tr_b16 v[208:209], v232 offset:53248\n"
    "ds_read_b64_tr_b16 v[210:211], v232 offset:53504\n"
    "v_mfma_f32_32x32x16_bf16 v[32:47], v[216:219], v[172:175], v[32:47]\n"
    "ds_read_b64_tr_b16 v[212:213], v232 offset:53760\n"
    "ds_read_b64_tr_b16 v[214:215], v232 offset:54016\n"
    "s_waitcnt lgkmcnt(15)\n"
    "v_mfma_f32_32x32x16_bf16 v[48:63], v[220:223], v[172:175], v[48:63]\n"
    "ds_read_b64_tr_b16 v[216:217], v232 offset:54272\n"
    "ds_read_b64_tr_b16 v[218:219], v232 offset:54528\n"
    "s_waitcnt lgkmcnt(9)\n"
    "v_mfma_f32_32x32x16_bf16 v[0:15], v[192:195], v[176:179], v[0:15]\n"
    "ds_read_b64_tr_b16 v[220:221], v232 offset:54784\n"
    "ds_read_b64_tr_b16 v[222:223], v232 offset:55040\n"
    "ds_write_b32 v226, v240 offset:32\n"
    "v_add_u32 v240, 1, v240\n"
    "v_mfma_f32_32x32x16_bf16 v[16:31], v[196:199], v[176:179], v[16:31]\n"
    "ds_read_b64_tr_b16 v[192:193], v232 offset:57344\n"
    "ds_read_b64_tr_b16 v[194:195], v232 offset:57600\n"
    "buffer_load_dwordx2 v[242:243], off, s[52:55], 0 sc1\n"
    "v_mfma_f32_32x32x16_bf16 v[32:47], v[200:203], v[176:179], v[32:47]\n"
    "ds_read_b64_tr_b16 v[196:197], v232 offset:57856\n"
    "ds_read_b64_tr_b16 v[198:199], v232 offset:58112\n"
    "v_mfma_f32_32x32x16_bf16 v[48:63], v[204:207], v[176:179], v[48:63]\n"
    "ds_read_b64_tr_b16 v[200:201], v232 offset:58368\n"
    "ds_read_b64_tr_b16 v[202:203], v232 offset:58624\n"
    "s_waitcnt lgkmcnt(13)\n"
    "v_mfma_f32_32x32x16_bf16 v[0:15], v[208:211], v[180:183], v[0:15]\n"
    "ds_read_b64_tr_b16 v[204:205], v232 offset:58880\n"
    "ds_read_b64_tr_b16 v[206:207], v232 offset:59136\n"
    "s_waitcnt lgkmcnt(13)\n"
    "v_mfma_f32_32x32x16_bf16 v[16:31], v[212:215], v[180:183], v[16:31]\n"
    "ds_read_b64_tr_b16 v[208:209], v232 offset:61440\n"
    "ds_read_b64_tr_b16 v[210:211], v232 offset:61696\n"
    "s_waitcnt lgkmcnt(13)\n"
    "v_mfma_f32_32x32x16_bf16 v[32:47], v[216:219], v[180:183], v[32:47]\n"
    "ds_read_b64_tr_b16 v[212:213], v232 offset:61952\n"
    "ds_read_b64_tr_b16 v[214:215], v232 offset:62208\n"
    "s_waitcnt lgkmcnt(13)\n"
    "v_mfma_f32_32x32x16_bf16 v[48:63], v[220:223], v[180:183], v[48:63]\n"
    "ds_read_b64_tr_b16 v[216:217], v232 offset:62464\n"
    "ds_read_b64_tr_b16 v[218:219], v232 offset:62720\n"
    "s_waitcnt lgkmcnt(12)\n"
    "v_mfma_f32_32x32x16_bf16 v[0:15], v[192:195], v[184:187], v[0:15]\n"
    "ds_read_b64_tr_b16 v[220:221], v232 offset:62976\n"
    "ds_read_b64_tr_b16 v[222:223], v232 offset:63232\n"
    "ds_write_b32 v226, v240 offset:32\n"
    "v_add_u32 v240, 1, v240\n"
    "s_waitcnt lgkmcnt(13)\n"
    "v_mfma_f32_32x32x16_bf16 v[16:31], v[196:199], v[184:187], v[16:31]\n"
    "s_waitcnt lgkmcnt(11)\n"
    "v_mfma_f32_32x32x16_bf16 v[32:47], v[200:203], v[184:187], v[32:47]\n"
    "s_waitcnt lgkmcnt(9)\n"
    "v_mfma_f32_32x32x16_bf16 v[48:63], v[204:207], v[184:187], v[48:63]\n"
    "s_waitcnt lgkmcnt(7)\n"
    "v_mfma_f32_32x32x16_bf16 v[0:15], v[208:211], v[188:191], v[0:15]\n"
    "s_waitcnt lgkmcnt(5)\n"
    "v_mfma_f32_32x32x16_bf16 v[16:31], v[212:215], v[188:191], v[16:31]\n"
    "s_waitcnt lgkmcnt(3)\n"
    "v_mfma_f32_32x32x16_bf16 v[32:47], v[216:219], v[188:191], v[32:47]\n"
    "s_waitcnt lgkmcnt(1)\n"
    "v_mfma_f32_32x32x16_bf16 v[48:63], v[220:223], v[188:191], v[48:63]\n"
    "s_mov_b32 s74, 17\n"
    "v_mov_b32 v230, v227\n"
    "v_mov_b32 v231, v228\n"
    "v_lshrrev_b32 v237, 5, v224\n"
    "v_mov_b32 v241, v229\n"
    "v_and_b32 v236, 30, v224\n"
    "v_lshlrev_b32 v236, 1, v236\n"
    "v_lshl_or_b32 v247, v237, 12, v236\n"
    "v_and_b32 v236, 1, v224\n"
    "v_lshl_or_b32 v247, v236, 6, v247\n"
    "v_add_u32 v246, 0x10000, v241\n"
    "v_add_u32 v248, 0x20000, v241\n"
    "s_mov_b32 s74, 18\n"
    "s_cmp_ge_u32 s72, s74\n"
    "s_cbranch_scc0 .Lmha_AQ0S\n"
    ".Lmha_AQ0:\n"
    "ds_read_b128 v[160:163], v230 offset:0\n"
    "ds_read_b128 v[164:167], v231 offset:0\n"
    "ds_read_b128 v[168:171], v230 offset:512\n"
    "ds_read_b128 v[172:175], v231 offset:512\n"
    "ds_read_b128 v[176:179], v230 offset:1024\n"
    "ds_read_b128 v[180:183], v231 offset:1024\n"
    "ds_read_b128 v[184:187], v230 offset:1536\n"
    "ds_read_b128 v[188:191], v231 offset:1536\n"
    "v_pk_mul_f32 v[0:1], v[0:1], s[92:93] op_sel_hi:[1,0]\n"
    "v_pk_mul_f32 v[2:3], v[2:3], s[92:93] op_sel_hi:[1,0]\n"
    "v_pk_mul_f32 v[4:5], v[4:5], s[92:93] op_sel_hi:[1,0]\n"
    "v_pk_mul_f32 v[6:7], v[6:7], s[92:93] op_sel_hi:[1,0]\n"
    "v_pk_mul_f32 v[8:9], v[8:9], s[92:93] op_sel_hi:[1,0]\n"
    "v_pk_mul_f32 v[10:11], v[10:11], s[92:93] op_sel_hi:[1,0]\n"
    "v_pk_mul_f32 v[12:13], v[12:13], s[92:93] op_sel_hi:[1,0]\n"
    "v_pk_mul_f32 v[14:15], v[14:15], s[92:93] op_sel_hi:[1,0]\n"
    "v_cvt_pk_bf16_f32 v128, v0, v1\n"
    "v_cvt_pk_bf16_f32 v129, v2, v3\n"
    "v_cvt_pk_bf16_f32 v130, v4, v5\n"
    "v_cvt_pk_bf16_f32 v131, v6, v7\n"
    "v_cvt_pk_bf16_f32 v132, v8, v9\n"
    "v_cvt_pk_bf16_f32 v133, v10, v11\n"
    "v_cvt_pk_bf16_f32 v134, v12, v13\n"
    "v_cvt_pk_bf16_f32 v135, v14, v15\n"
    "v_mov_b64 v[0:1], 0\n"
    "v_mov_b64 v[2:3], 0\n"
    "s_waitcnt lgkmcnt(7)\n"
    "v_mfma_f32_32x32x16_bf16 v[64:79], v[160:163], v[128:131], 0\n"
    "v_mov_b64 v[4:5], 0\n"
    "v_mov_b64 v[6:7], 0\n"
    "v_mov_b64 v[8:9], 0\n"
    "v_mov_b64 v[10:11], 0\n"
    "v_mov_b64 v[12:13], 0\n"
    "v_mov_b64 v[14:15], 0\n"
    "v_pk_mul_f32 v[16:17], v[16:17], s[92:93] op_sel_hi:[1,0]\n"
    "v_pk_mul_f32 v[18:19], v[18:19], s[92:93] op_sel_hi:[1,0]\n"
    "v_pk_mul_f32 v[20:21], v[20:21], s[92:93] op_sel_hi:[1,0]\n"
    "v_pk_mul_f32 v[22:23], v[22:23], s[92:93] op_sel_hi:[1,0]\n"
    "v_pk_mul_f32 v[24:25], v[24:25], s[92:93] op_sel_hi:[1,0]\n"
    "v_pk_mul_f32 v[26:27], v[26:27], s[92:93] op_sel_hi:[1,0]\n"
    "v_pk_mul_f32 v[28:29], v[28:29], s[92:93] op_sel_hi:[1,0]\n"
    "v_pk_mul_f32 v[30:31], v[30:31], s[92:93] op_sel_hi:[1,0]\n"
    "s_waitcnt lgkmcnt(6)\n"
    "v_mfma_f32_32x32x16_bf16 v[64:79], v[164:167], v[132:135], v[64:79]\n"
    "ds_read_b128 v[160:163], v230 offset:8192\n"
    "v_cvt_pk_bf16_f32 v136, v16, v17\n"
    "v_cvt_pk_bf16_f32 v137, v18, v19\n"
    "v_cvt_pk_bf16_f32 v138, v20, v21\n"
    "v_cvt_pk_bf16_f32 v139, v22, v23\n"
    "v_cvt_pk_bf16_f32 v140, v24, v25\n"
    "v_cvt_pk_bf16_f32 v141, v26, v27\n"
    "v_cvt_pk_bf16_f32 v142, v28, v29\n"
    "v_cvt_pk_bf16_f32 v143, v30, v31\n"
    "v_mov_b64 v[16:17], 0\n"
    "v_mov_b64 v[18:19], 0\n"
    "s_waitcnt lgkmcnt(6)\n"
    "v_mfma_f32_32x32x16_bf16 v[64:79], v[168:171], v[136:139], v[64:79]\n"
    "ds_read_b128 v[164:167], v231 offset:8192\n"
    "v_mov_b64 v[20:21], 0\n"
    "v_mov_b64 v[22:23], 0\n"
    "v_mov_b64 v[24:25], 0\n"
    "v_mov_b64 v[26:27], 0\n"
    "v_mov_b64 v[28:29], 0\n"
    "v_mov_b64 v[30:31], 0\n"
    "v_pk_mul_f32 v[32:33], v[32:33], s[92:93] op_sel_hi:[1,0]\n"
    "v_pk_mul_f32 v[34:35], v[34:35], s[92:93] op_sel_hi:[1,0]\n"
    "v_pk_mul_f32 v[36:37], v[36:37], s[92:93] op_sel_hi:[1,0]\n"
    "v_pk_mul_f32 v[38:39], v[38:39], s[92:93] op_sel_hi:[1,0]\n"
    "v_pk_mul_f32 v[40:41], v[40:41], s[92:93] op_sel_hi:[1,0]\n"
    "v_pk_mul_f32 v[42:43], v[42:43], s[92:93] op_sel_hi:[1,0]\n"
    "v_pk_mul_f32 v[44:45], v[44:45], s[92:93] op_sel_hi:[1,0]\n"
    "v_pk_mul_f32 v[46:47], v[46:47], s[92:93] op_sel_hi:[1,0]\n"
    "s_waitcnt lgkmcnt(6)\n"
    "v_mfma_f32_32x32x16_bf16 v[64:79], v[172:175], v[140:143], v[64:79]\n"
    "ds_read_b128 v[168:171], v230 offset:8704\n"
    "v_cvt_pk_bf16_f32 v144, v32, v33\n"
    "v_cvt_pk_bf16_f32 v145, v34, v35\n"
    "v_cvt_pk_bf16_f32 v146, v36, v37\n"
    "v_cvt_pk_bf16_f32 v147, v38, v39\n"
    "v_cvt_pk_bf16_f32 v148, v40, v41\n"
    "v_cvt_pk_bf16_f32 v149, v42, v43\n"
    "v_cvt_pk_bf16_f32 v150, v44, v45\n"
    "v_cvt_pk_bf16_f32 v151, v46, v47\n"
    "v_mov_b64 v[32:33], 0\n"
    "v_mov_b64 v[34:35], 0\n"
    "s_waitcnt lgkmcnt(6)\n"
    "v_mfma_f32_32x32x16_bf16 v[64:79], v[176:179], v[144:147], v[64:79]\n"
    "ds_read_b128 v[172:175], v231 offset:8704\n"
    "v_mov_b64 v[36:37], 0\n"
    "v_mov_b64 v[38:39], 0\n"
    "v_mov_b64 v[40:41], 0\n"
    "v_mov_b64 v[42:43], 0\n"
    "v_mov_b64 v[44:45], 0\n"
    "v_mov_b64 v[46:47], 0\n"
    "v_pk_mul_f32 v[48:49], v[48:49], s[92:93] op_sel_hi:[1,0]\n"
    "v_pk_mul_f32 v[50:51], v[50:51], s[92:93] op_sel_hi:[1,0]\n"
    "v_pk_mul_f32 v[52:53], v[52:53], s[92:93] op_sel_hi:[1,0]\n"
    "v_pk_mul_f32 v[54:55], v[54:55], s[92:93] op_sel_hi:[1,0]\n"
    "v_pk_mul_f32 v[56:57], v[56:57], s[92:93] op_sel_hi:[1,0]\n"
    "v_pk_mul_f32 v[58:59], v[58:59], s[92:93] op_sel_hi:[1,0]\n"
    "v_pk_mul_f32 v[60:61], v[60:61], s[92:93] op_sel_hi:[1,0]\n"
    "v_pk_mul_f32 v[62:63], v[62:63], s[92:93] op_sel_hi:[1,0]\n"
    "s_waitcnt lgkmcnt(6)\n"
    "v_mfma_f32_32x32x16_bf16 v[64:79], v[180:183], v[148:151], v[64:79]\n"
    "ds_read_b128 v[176:179], v230 offset:9216\n"
    "v_cvt_pk_bf16_f32 v152, v48, v49\n"
    "v_cvt_pk_bf16_f32 v153, v50, v51\n"
    "v_cvt_pk_bf16_f32 v154, v52, v53\n"
    "v_cvt_pk_bf16_f32 v155, v54, v55\n"
    "v_cvt_pk_bf16_f32 v156, v56, v57\n"
    "v_cvt_pk_bf16_f32 v157, v58, v59\n"
    "v_cvt_pk_bf16_f32 v158, v60, v61\n"
    "v_cvt_pk_bf16_f32 v159, v62, v63\n"
    "v_mov_b64 v[48:49], 0\n"
    "v_mov_b64 v[50:51], 0\n"
    "s_waitcnt lgkmcnt(6)\n"
    "v_mfma_f32_32x32x16_bf16 v[64:79], v[184:187], v[152:155], v[64:79]\n"
    "ds_read_b128 v[180:183], v231 offset:9216\n"
    "v_mov_b64 v[52:53], 0\n"
    "v_mov_b64 v[54:55], 0\n"
    "v_mov_b64 v[56:57], 0\n"
    "v_mov_b64 v[58:59], 0\n"
    "v_mov_b64 v[60:61], 0\n"
    "v_mov_b64 v[62:63], 0\n"
    "s_waitcnt lgkmcnt(6)\n"
    "v_mfma_f32_32x32x16_bf16 v[64:79], v[188:191], v[156:159], v[64:79]\n"
    "ds_read_b128 v[184:187], v230 offset:9728\n"
    "ds_read_b128 v[188:191], v231 offset:9728\n"
    "s_nop 7\n"
    "v_mov_b32 v240, 18\n"
    "ds_write_b32 v226, v240 offset:32\n"
    "v_max3_f32 v236, v64, v65, v66\n"
    "v_max3_f32 v237, v67, v68, v69\n"
    "v_max3_f32 v236, v236, v70, v71\n"
    "v_max3_f32 v237, v237, v72, v73\n"
    "v_max3_f32 v236, v236, v74, v75\n"
    "v_max3_f32 v237, v237, v76, v77\n"
    "v_max3_f32 v236, v236, v78, v79\n"
    "v_max_f32 v236, v236, v237\n"
    "v_mov_b32 v237, v236\n"
    "v_mov_b32 v233, 0\n"
    "v_mov_b32 v234, 0\n"
    "v_permlane32_swap_b32 v236, v237\n"
    "v_max_f32 v236, v236, v237\n"
    "v_pk_add_f32 v[64:65], v[64:65], v[236:237] op_sel_hi:[1,0] neg_lo:[0,1] neg_hi:[0,1]\n"
    "v_pk_add_f32 v[66:67], v[66:67], v[236:237] op_sel_hi:[1,0] neg_lo:[0,1] neg_hi:[0,1]\n"
    "v_pk_add_f32 v[68:69], v[68:69], v[236:237] op_sel_hi:[1,0] neg_lo:[0,1] neg_hi:[0,1]\n"
    "v_pk_add_f32 v[70:71], v[70:71], v[236:237] op_sel_hi:[1,0] neg_lo:[0,1] neg_hi:[0,1]\n"
    "v_pk_add_f32 v[72:73], v[72:73], v[236:237] op_sel_hi:[1,0] neg_lo:[0,1] neg_hi:[0,1]\n"
    "v_pk_add_f32 v[74:75], v[74:75], v[236:237] op_sel_hi:[1,0] neg_lo:[0,1] neg_hi:[0,1]\n"
    "v_pk_add_f32 v[76:77], v[76:77], v[236:237] op_sel_hi:[1,0] neg_lo:[0,1] neg_hi:[0,1]\n"
    "v_pk_add_f32 v[78:79], v[78:79], v[236:237] op_sel_hi:[1,0] neg_lo:[0,1] neg_hi:[0,1]\n"
    "v_xor_b32 v96, 0x80000000, v236\n"
    "v_xor_b32 v97, 0x80000000, v236\n"
    "v_mov_b64 v[98:99], v[96:97]\n"
    "v_mov_b64 v[100:101], v[96:97]\n"
    "v_mov_b64 v[102:103], v[96:97]\n"
    "v_mov_b64 v[104:105], v[96:97]\n"
    "v_mov_b64 v[106:107], v[96:97]\n"
    "v_mov_b64 v[108:109], v[96:97]\n"
    "v_mov_b64 v[110:111], v[96:97]\n"
    "s_mov_b32 s74, 19\n"
    "s_mov_b32 s75, 16384\n"
    ".Lmha_LOOP:\n"
    "s_cmp_ge_u32 s72, s74\n"
    "s_cbranch_scc0 .Lmha_AQV0S\n"
    ".Lmha_AQV0:\n"
    "v_add_u32 v232, s75, v229\n"
    "s_waitcnt lgkmcnt(0)\n"
    "v_mfma_f32_32x32x16_bf16 v[80:95], v[160:163], v[128:131], v[96:111]\n"
    "ds_read_b64_tr_b16 v[192:193], v232 offset:0\n"
    "ds_read_b64_tr_b16 v[194:195], v232 offset:2048\n"
    "v_exp_f32 v64, v64\n"
    "v_exp_f32 v65, v65\n"
    "s_add_u32 s74, s74, 1\n"
    "v_mfma_f32_32x32x16_bf16 v[80:95], v[164:167], v[132:135], v[80:95]\n"
    "ds_read_b64_tr_b16 v[196:197], v232 offset:512\n"
    "ds_read_b64_tr_b16 v[198:199], v232 offset:2560\n"
    "v_exp_f32 v66, v66\n"
    "v_exp_f32 v67, v67\n"
    "v_cvt_pk_bf16_f32 v112, v64, v65\n"
    "v_mfma_f32_32x32x16_bf16 v[80:95], v[168:171], v[136:139], v[80:95]\n"
    "ds_read_b64_tr_b16 v[200:201], v232 offset:1024\n"
    "ds_read_b64_tr_b16 v[202:203], v232 offset:3072\n"
    "v_exp_f32 v68, v68\n"
    "v_exp_f32 v69, v69\n"
    "v_cvt_pk_bf16_f32 v113, v66, v67\n"
    "v_mfma_f32_32x32x16_bf16 v[80:95], v[172:175], v[140:143], v[80:95]\n"
    "ds_read_b64_tr_b16 v[204:205], v232 offset:1536\n"
    "ds_read_b64_tr_b16 v[206:207], v232 offset:3584\n"
    "v_exp_f32 v70, v70\n"
    "v_exp_f32 v71, v71\n"
    "v_cvt_pk_bf16_f32 v114, v68, v69\n"
    "v_mfma_f32_32x32x16_bf16 v[80:95], v[176:179], v[144:147], v[80:95]\n"
    "ds_read_b64_tr_b16 v[208:209], v232 offset:4096\n"
    "ds_read_b64_tr_b16 v[210:211], v232 offset:6144\n"
    "v_exp_f32 v72, v72\n"
    "v_exp_f32 v73, v73\n"
    "v_cvt_pk_bf16_f32 v115, v70, v71\n"
    "v_mfma_f32_32x32x16_bf16 v[80:95], v[180:183], v[148:151], v[80:95]\n"
    "ds_read_b64_tr_b16 v[212:213], v232 offset:4608\n"
    "ds_read_b64_tr_b16 v[214:215], v232 offset:6656\n"
    "v_exp_f32 v74, v74\n"
    "v_exp_f32 v75, v75\n"
    "v_cvt_pk_bf16_f32 v116, v72, v73\n"
    "v_mfma_f32_32x32x16_bf16 v[80:95], v[184:187], v[152:155], v[80:95]\n"
    "ds_read_b64_tr_b16 v[216:217], v232 offset:5120\n"
    "ds_read_b64_tr_b16 v[218:219], v232 offset:7168\n"
    "v_exp_f32 v76, v76\n"
    "v_exp_f32 v77, v77\n"
    "v_cvt_pk_bf16_f32 v117, v74, v75\n"
    "v_mfma_f32_32x32x16_bf16 v[80:95], v[188:191], v[156:159], v[80:95]\n"
    "ds_read_b64_tr_b16 v[220:221], v232 offset:5632\n"
    "ds_read_b64_tr_b16 v[222:223], v232 offset:7680\n"
    "v_exp_f32 v78, v78\n"
    "v_exp_f32 v79, v79\n"
    "v_cvt_pk_bf16_f32 v118, v76, v77\n"
    "v_add_u32 v230, s75, v227\n"
    "v_cvt_pk_bf16_f32 v119, v78, v79\n"
    "v_add_u32 v231, s75, v228\n"
    "s_cmp_ge_u32 s72, s74\n"
    "s_cbranch_scc0 .Lmha_AQK0S\n"
    ".Lmha_AQK0:\n"
    "s_waitcnt lgkmcnt(8)\n"
    "v_mfma_f32_32x32x16_bf16 v[0:15], v[192:195], v[112:115], v[0:15]\n"
    "ds_read_b128 v[160:163], v230 offset:8192\n"
    "ds_read_b128 v[164:167], v231 offset:8192\n"
    "v_add_f32 v233, v233, v64\n"
    "v_add_f32 v234, v234, v65\n"
    "s_add_u32 s74, s74, 1\n"
    "v_mfma_f32_32x32x16_bf16 v[16:31], v[196:199], v[112:115], v[16:31]\n"
    "ds_read_b128 v[168:171], v230 offset:8704\n"
    "v_add_f32 v233, v233, v66\n"
    "v_add_f32 v234, v234, v67\n"
    "s_add_u32 s75, s75, 16384\n"
    "v_max3_f32 v236, v80, v81, v82\n"
    "v_mfma_f32_32x32x16_bf16 v[32:47], v[200:203], v[112:115], v[32:47]\n"
    "ds_read_b128 v[172:175], v231 offset:8704\n"
    "v_add_f32 v233, v233, v68\n"
    "v_add_f32 v234, v234, v69\n"
    "s_and_b32 s75, s75, s77\n"
    "v_max3_f32 v237, v83, v84, v85\n"
    "v_mfma_f32_32x32x16_bf16 v[48:63], v[204:207], v[112:115], v[48:63]\n"
    "ds_read_b128 v[176:179], v230 offset:9216\n"
    "v_add_f32 v233, v233, v70\n"
    "v_add_f32 v234, v234, v71\n"
    "v_add_u32 v240, 2, v240\n"
    "v_max3_f32 v236, v236, v86, v87\n"
    "s_waitcnt lgkmcnt(5)\n"
    "v_mfma_f32_32x32x16_bf16 v[0:15], v[208:211], v[116:119], v[0:15]\n"
    "ds_read_b128 v[180:183], v231 offset:9216\n"
    "v_add_f32 v233, v233, v72\n"
    "v_add_f32 v234, v234, v73\n"
    "v_max3_f32 v237, v237, v88, v89\n"
    "v_mfma_f32_32x32x16_bf16 v[16:31], v[212:215], v[116:119], v[16:31]\n"
    "ds_read_b128 v[184:187], v230 offset:9728\n"
    "v_add_f32 v233, v233, v74\n"
    "v_add_f32 v234, v234, v75\n"
    "v_max3_f32 v236, v236, v90, v91\n"
    "v_mfma_f32_32x32x16_bf16 v[32:47], v[216:219], v[116:119], v[32:47]\n"
    "ds_read_b128 v[188:191], v231 offset:9728\n"
    "v_add_f32 v233, v233, v76\n"
    "v_add_f32 v234, v234, v77\n"
    "v_max3_f32 v237, v237, v92, v93\n"
    "v_mfma_f32_32x32x16_bf16 v[48:63], v[220:223], v[116:119], v[48:63]\n"
    "ds_write_b32 v226, v240 offset:32\n"
    "v_add_f32 v233, v233, v78\n"
    "v_add_f32 v234, v234, v79\n"
    "v_max3_f32 v236, v236, v94, v95\n"
    "v_max_f32 v236, v236, v237\n"
    "v_cmp_lt_f32 vcc, s78, v236\n"
    "s_cbranch_vccnz .Lmha_RARE0\n"
    ".Lmha_BACK0:\n"
    "s_cmp_ge_u32 s72, s74\n"
    "s_cbranch_scc0 .Lmha_AQV1S\n"
    ".Lmha_AQV1:\n"
    "v_add_u32 v232, s75, v229\n"
    "s_waitcnt lgkmcnt(0)\n"
    "v_mfma_f32_32x32x16_bf16 v[64:79], v[160:163], v[128:131], v[96:111]\n"
    "ds_read_b64_tr_b16 v[192:193], v232 offset:0\n"
    "ds_read_b64_tr_b16 v[194:195], v232 offset:2048\n"
    "v_exp_f32 v80, v80\n"
    "v_exp_f32 v81, v81\n"
    "s_add_u32 s74, s74, 1\n"
    "v_mfma_f32_32x32x16_bf16 v[64:79], v[164:167], v[132:135], v[64:79]\n"
    "ds_read_b64_tr_b16 v[196:197], v232 offset:512\n"
    "ds_read_b64_tr_b16 v[198:199], v232 offset:2560\n"
    "v_exp_f32 v82, v82\n"
    "v_exp_f32 v83, v83\n"
    "v_cvt_pk_bf16_f32 v112, v80, v81\n"
    "v_mfma_f32_32x32x16_bf16 v[64:79], v[168:171], v[136:139], v[64:79]\n"
    "ds_read_b64_tr_b16 v[200:201], v232 offset:1024\n"
    "ds_read_b64_tr_b16 v[202:203], v232 offset:3072\n"
    "v_exp_f32 v84, v84\n"
    "v_exp_f32 v85, v85\n"
    "v_cvt_pk_bf16_f32 v113, v82, v83\n"
    "v_mfma_f32_32x32x16_bf16 v[64:79], v[172:175], v[140:143], v[64:79]\n"
    "ds_read_b64_tr_b16 v[204:205], v232 offset:1536\n"
    "ds_read_b64_tr_b16 v[206:207], v232 offset:3584\n"
    "v_exp_f32 v86, v86\n"
    "v_exp_f32 v87, v87\n"
    "v_cvt_pk_bf16_f32 v114, v84, v85\n"
    "v_mfma_f32_32x32x16_bf16 v[64:79], v[176:179], v[144:147], v[64:79]\n"
    "ds_read_b64_tr_b16 v[208:209], v232 offset:4096\n"
    "ds_read_b64_tr_b16 v[210:211], v232 offset:6144\n"
    "v_exp_f32 v88, v88\n"
    "v_exp_f32 v89, v89\n"
    "v_cvt_pk_bf16_f32 v115, v86, v87\n"
    "v_mfma_f32_32x32x16_bf16 v[64:79], v[180:183], v[148:151], v[64:79]\n"
    "ds_read_b64_tr_b16 v[212:213], v232 offset:4608\n"
    "ds_read_b64_tr_b16 v[214:215], v232 offset:6656\n"
    "v_exp_f32 v90, v90\n"
    "v_exp_f32 v91, v91\n"
    "v_cvt_pk_bf16_f32 v116, v88, v89\n"
    "v_mfma_f32_32x32x16_bf16 v[64:79], v[184:187], v[152:155], v[64:79]\n"
    "ds_read_b64_tr_b16 v[216:217], v232 offset:5120\n"
    "ds_read_b64_tr_b16 v[218:219], v232 offset:7168\n"
    "v_exp_f32 v92, v92\n"
    "v_exp_f32 v93, v93\n"
    "v_cvt_pk_bf16_f32 v117, v90, v91\n"
    "v_mfma_f32_32x32x16_bf16 v[64:79], v[188:191], v[156:159], v[64:79]\n"
    "ds_read_b64_tr_b16 v[220:221], v232 offset:5632\n"
    "ds_read_b64_tr_b16 v[222:223], v232 offset:7680\n"
    "v_exp_f32 v94, v94\n"
    "v_exp_f32 v95, v95\n"
    "v_cvt_pk_bf16_f32 v118, v92, v93\n"
    "v_add_u32 v230, s75, v227\n"
    "v_cvt_pk_bf16_f32 v119, v94, v95\n"
    "v_add_u32 v231, s75, v228\n"
    "s_cmp_ge_u32 s72, s74\n"
    "s_cbranch_scc0 .Lmha_AQK1S\n"
    ".Lmha_AQK1:\n"
    "s_waitcnt lgkmcnt(8)\n"
    "v_mfma_f32_32x32x16_bf16 v[0:15], v[192:195], v[112:115], v[0:15]\n"
    "ds_read_b128 v[160:163], v230 offset:8192\n"
    "ds_read_b128 v[164:167], v231 offset:8192\n"
    "v_add_f32 v233, v233, v80\n"
    "v_add_f32 v234, v234, v81\n"
    "s_add_u32 s74, s74, 1\n"
    "v_mfma_f32_32x32x16_bf16 v[16:31], v[196:199], v[112:115], v[16:31]\n"
    "ds_read_b128 v[168:171], v230 offset:8704\n"
    "v_add_f32 v233, v233, v82\n"
    "v_add_f32 v234, v234, v83\n"
    "s_add_u32 s75, s75, 16384\n"
    "v_max3_f32 v236, v64, v65, v66\n"
    "v_mfma_f32_32x32x16_bf16 v[32:47], v[200:203], v[112:115], v[32:47]\n"
    "ds_read_b128 v[172:175], v231 offset:8704\n"
    "v_add_f32 v233, v233, v84\n"
    "v_add_f32 v234, v234, v85\n"
    "s_and_b32 s75, s75, s77\n"
    "v_max3_f32 v237, v67, v68, v69\n"
    "v_mfma_f32_32x32x16_bf16 v[48:63], v[204:207], v[112:115], v[48:63]\n"
    "ds_read_b128 v[176:179], v230 offset:9216\n"
    "v_add_f32 v233, v233, v86\n"
    "v_add_f32 v234, v234, v87\n"
    "v_add_u32 v240, 2, v240\n"
    "v_max3_f32 v236, v236, v70, v71\n"
    "s_waitcnt lgkmcnt(5)\n"
    "v_mfma_f32_32x32x16_bf16 v[0:15], v[208:211], v[116:119], v[0:15]\n"
    "ds_read_b128 v[180:183], v231 offset:9216\n"
    "v_add_f32 v233, v233, v88\n"
    "v_add_f32 v234, v234, v89\n"
    "v_max3_f32 v237, v237, v72, v73\n"
    "v_mfma_f32_32x32x16_bf16 v[16:31], v[212:215], v[116:119], v[16:31]\n"
    "ds_read_b128 v[184:187], v230 offset:9728\n"
    "v_add_f32 v233, v233, v90\n"
    "v_add_f32 v234, v234, v91\n"
    "v_max3_f32 v236, v236, v74, v75\n"
    "v_mfma_f32_32x32x16_bf16 v[32:47], v[216:219], v[116:119], v[32:47]\n"
    "ds_read_b128 v[188:191], v231 offset:9728\n"
    "v_add_f32 v233, v233, v92\n"
    "v_add_f32 v234, v234, v93\n"
    "v_max3_f32 v237, v237, v76, v77\n"
    "v_mfma_f32_32x32x16_bf16 v[48:63], v[220:223], v[116:119], v[48:63]\n"
    "ds_write_b32 v226, v240 offset:32\n"
    "v_add_f32 v233, v233, v94\n"
    "v_add_f32 v234, v234, v95\n"
    "v_max3_f32 v236, v236, v78, v79\n"
    "v_max_f32 v236, v236, v237\n"
    "v_cmp_lt_f32 vcc, s78, v236\n"
    "s_cbranch_vccnz .Lmha_RARE1\n"
    ".Lmha_BACK1:\n"
    "s_cmp_eq_u32 s74, 75\n"
    "s_cbranch_scc1 .Lmha_CLR\n"
    ".Lmha_CLRX:\n"
    "s_cmp_lt_u32 s74, 82\n"
    "s_cbranch_scc1 .Lmha_LOOP\n"
    "buffer_load_dwordx2 v[254:255], v244, s[52:55], s88 offen sc1\n"
    "v_add_f32 v236, v233, v234\n"
    "v_mov_b32 v237, v236\n"
    "s_waitcnt lgkmcnt(0)\n"
    "s_nop 0\n"
    "v_permlane32_swap_b32 v236, v237\n"
    "v_add_f32 v236, v236, v237\n"
    "v_rcp_f32 v236, v236\n"
    "s_add_u32 s60, s94, 0x1000\n"
    "v_pk_mul_f32 v[0:1], v[0:1], v[236:237] op_sel_hi:[1,0]\n"
    "v_pk_mul_f32 v[2:3], v[2:3], v[236:237] op_sel_hi:[1,0]\n"
    "v_pk_mul_f32 v[4:5], v[4:5], v[236:237] op_sel_hi:[1,0]\n"
    "v_pk_mul_f32 v[6:7], v[6:7], v[236:237] op_sel_hi:[1,0]\n"
    "v_cvt_pk_bf16_f32 v64, v0, v1\n"
    "v_cvt_pk_bf16_f32 v65, v2, v3\n"
    "v_cvt_pk_bf16_f32 v66, v4, v5\n"
    "v_cvt_pk_bf16_f32 v67, v6, v7\n"
    "buffer_store_dwordx4 v[64:67], v239, s[52:55], s94 offen offset:0 sc1\n"
    "v_pk_mul_f32 v[8:9], v[8:9], v[236:237] op_sel_hi:[1,0]\n"
    "v_pk_mul_f32 v[10:11], v[10:11], v[236:237] op_sel_hi:[1,0]\n"
    "v_pk_mul_f32 v[12:13], v[12:13], v[236:237] op_sel_hi:[1,0]\n"
    "v_pk_mul_f32 v[14:15], v[14:15], v[236:237] op_sel_hi:[1,0]\n"
    "v_cvt_pk_bf16_f32 v68, v8, v9\n"
    "v_cvt_pk_bf16_f32 v69, v10, v11\n"
    "v_cvt_pk_bf16_f32 v70, v12, v13\n"
    "v_cvt_pk_bf16_f32 v71, v14, v15\n"
    "buffer_store_dwordx4 v[68:71], v239, s[52:55], s94 offen offset:1024 sc1\n"
    "v_pk_mul_f32 v[16:17], v[16:17], v[236:237] op_sel_hi:[1,0]\n"
    "v_pk_mul_f32 v[18:19], v[18:19], v[236:237] op_sel_hi:[1,0]\n"
    "v_pk_mul_f32 v[20:21], v[20:21], v[236:237] op_sel_hi:[1,0]\n"
    "v_pk_mul_f32 v[22:23], v[22:23], v[236:237] op_sel_hi:[1,0]\n"
    "v_cvt_pk_bf16_f32 v72, v16, v17\n"
    "v_cvt_pk_bf16_f32 v73, v18, v19\n"
    "v_cvt_pk_bf16_f32 v74, v20, v21\n"
    "v_cvt_pk_bf16_f32 v75, v22, v23\n"
    "buffer_store_dwordx4 v[72:75], v239, s[52:55], s94 offen offset:2048 sc1\n"
    "v_pk_mul_f32 v[24:25], v[24:25], v[236:237] op_sel_hi:[1,0]\n"
    "v_pk_mul_f32 v[26:27], v[26:27], v[236:237] op_sel_hi:[1,0]\n"
    "v_pk_mul_f32 v[28:29], v[28:29], v[236:237] op_sel_hi:[1,0]\n"
    "v_pk_mul_f32 v[30:31], v[30:31], v[236:237] op_sel_hi:[1,0]\n"
    "v_cvt_pk_bf16_f32 v76, v24, v25\n"
    "v_cvt_pk_bf16_f32 v77, v26, v27\n"
    "v_cvt_pk_bf16_f32 v78, v28, v29\n"
    "v_cvt_pk_bf16_f32 v79, v30, v31\n"
    "buffer_store_dwordx4 v[76:79], v239, s[52:55], s94 offen offset:3072 sc1\n"
    "v_pk_mul_f32 v[32:33], v[32:33], v[236:237] op_sel_hi:[1,0]\n"
    "v_pk_mul_f32 v[34:35], v[34:35], v[236:237] op_sel_hi:[1,0]\n"
    "v_pk_mul_f32 v[36:37], v[36:37], v[236:237] op_sel_hi:[1,0]\n"
    "v_pk_mul_f32 v[38:39], v[38:39], v[236:237] op_sel_hi:[1,0]\n"
    "v_cvt_pk_bf16_f32 v80, v32, v33\n"
    "v_cvt_pk_bf16_f32 v81, v34, v35\n"
    "v_cvt_pk_bf16_f32 v82, v36, v37\n"
    "v_cvt_pk_bf16_f32 v83, v38, v39\n"
    "buffer_store_dwordx4 v[80:83], v239, s[52:55], s60 offen offset:0 sc1\n"
    "v_pk_mul_f32 v[40:41], v[40:41], v[236:237] op_sel_hi:[1,0]\n"
    "v_pk_mul_f32 v[42:43], v[42:43], v[236:237] op_sel_hi:[1,0]\n"
    "v_pk_mul_f32 v[44:45], v[44:45], v[236:237] op_sel_hi:[1,0]\n"
    "v_pk_mul_f32 v[46:47], v[46:47], v[236:237] op_sel_hi:[1,0]\n"
    "v_cvt_pk_bf16_f32 v84, v40, v41\n"
    "v_cvt_pk_bf16_f32 v85, v42, v43\n"
    "v_cvt_pk_bf16_f32 v86, v44, v45\n"
    "v_cvt_pk_bf16_f32 v87, v46, v47\n"
    "buffer_store_dwordx4 v[84:87], v239, s[52:55], s60 offen offset:1024 sc1\n"
    "v_pk_mul_f32 v[48:49], v[48:49], v[236:237] op_sel_hi:[1,0]\n"
    "v_pk_mul_f32 v[50:51], v[50:51], v[236:237] op_sel_hi:[1,0]\n"
    "v_pk_mul_f32 v[52:53], v[52:53], v[236:237] op_sel_hi:[1,0]\n"
    "v_pk_mul_f32 v[54:55], v[54:55], v[236:237] op_sel_hi:[1,0]\n"
    "v_cvt_pk_bf16_f32 v88, v48, v49\n"
    "v_cvt_pk_bf16_f32 v89, v50, v51\n"
    "v_cvt_pk_bf16_f32 v90, v52, v53\n"
    "v_cvt_pk_bf16_f32 v91, v54, v55\n"
    "buffer_store_dwordx4 v[88:91], v239, s[52:55], s60 offen offset:2048 sc1\n"
    "v_pk_mul_f32 v[56:57], v[56:57], v[236:237] op_sel_hi:[1,0]\n"
    "v_pk_mul_f32 v[58:59], v[58:59], v[236:237] op_sel_hi:[1,0]\n"
    "v_pk_mul_f32 v[60:61], v[60:61], v[236:237] op_sel_hi:[1,0]\n"
    "v_pk_mul_f32 v[62:63], v[62:63], v[236:237] op_sel_hi:[1,0]\n"
    "v_cvt_pk_bf16_f32 v92, v56, v57\n"
    "v_cvt_pk_bf16_f32 v93, v58, v59\n"
    "v_cvt_pk_bf16_f32 v94, v60, v61\n"
    "v_cvt_pk_bf16_f32 v95, v62, v63\n"
    "buffer_store_dwordx4 v[92:95], v239, s[52:55], s60 offen offset:3072 sc1\n"
    "v_mov_b32 v240, 999\n"
    "ds_write_b32 v226, v240 offset:32\n"
    "s_waitcnt vmcnt(8)\n"
    "v_readfirstlane_b32 s80, v242\n"
    "v_readfirstlane_b32 s81, v243\n"
    "s_add_u32 s80, s80, 0x7f4a7c15\n"
    "s_addc_u32 s81, s81, 0x9e3779b9\n"
    "v_mov_b64 v[242:243], s[80:81]\n"
    "v_cmp_ne_u64 vcc, s[80:81], v[254:255]\n"
    "s_mov_b32 s63, vcc_lo\n"
    "ds_read_b64_tr_b16 v[192:193], v248 offset:0\n"
    "ds_read_b64_tr_b16 v[194:195], v248 offset:1024\n"
    "ds_read_b64_tr_b16 v[196:197], v248 offset:512\n"
    "ds_read_b64_tr_b16 v[198:199], v248 offset:1536\n"
    "ds_read_b64_tr_b16 v[200:201], v248 offset:2048\n"
    "ds_read_b64_tr_b16 v[202:203], v248 offset:3072\n"
    "ds_read_b64_tr_b16 v[204:205], v248 offset:2560\n"
    "ds_read_b64_tr_b16 v[206:207], v248 offset:3584\n"
    "ds_read_b64_tr_b16 v[208:209], v248 offset:4096\n"
    "ds_read_b64_tr_b16 v[210:211], v248 offset:5120\n"
    "ds_read_b64_tr_b16 v[212:213], v248 offset:4608\n"
    "ds_read_b64_tr_b16 v[214:215], v248 offset:5632\n"
    "ds_read_b64_tr_b16 v[216:217], v248 offset:6144\n"
    "ds_read_b64_tr_b16 v[218:219], v248 offset:7168\n"
    "s_mov_b32 s73, 0\n"
    "s_and_b32 s60, s63, 7\n"
    "s_cbranch_scc0 .Lmha_EARLYA\n"
    "s_and_b32 s60, s63, 0x78\n"
    "s_cbranch_scc0 .Lmha_EARLYB\n"
    ".Lmha_SLOT0:\n"
    "s_waitcnt lgkmcnt(12)\n"
    "v_mfma_f32_32x32x16_bf16 v[0:15], v[64:67], v[192:195], 0\n"
    "ds_read_b64_tr_b16 v[220:221], v248 offset:6656\n"
    "ds_read_b64_tr_b16 v[222:223], v248 offset:7680\n"
    "s_waitcnt lgkmcnt(12)\n"
    "v_mfma_f32_32x32x16_bf16 v[16:31], v[64:67], v[196:199], 0\n"
    "ds_read_b64_tr_b16 v[192:193], v248 offset:8192\n"
    "ds_read_b64_tr_b16 v[194:195], v248 offset:9216\n"
    "s_waitcnt lgkmcnt(12)\n"
    "v_mfma_f32_32x32x16_bf16 v[0:15], v[68:71], v[200:203], v[0:15]\n"
    "ds_read_b64_tr_b16 v[196:197], v248 offset:8704\n"
    "ds_read_b64_tr_b16 v[198:199], v248 offset:9728\n"
    "s_waitcnt lgkmcnt(12)\n"
    "v_mfma_f32_32x32x16_bf16 v[16:31], v[68:71], v[204:207], v[16:31]\n"
    "ds_read_b64_tr_b16 v[200:201], v248 offset:10240\n"
    "ds_read_b64_tr_b16 v[202:203], v248 offset:11264\n"
    "s_waitcnt lgkmcnt(12)\n"
    "v_mfma_f32_32x32x16_bf16 v[0:15], v[72:75], v[208:211], v[0:15]\n"
    "ds_read_b64_tr_b16 v[204:205], v248 offset:10752\n"
    "ds_read_b64_tr_b16 v[206:207], v248 offset:11776\n"
    "s_waitcnt lgkmcnt(12)\n"
    "v_mfma_f32_32x32x16_bf16 v[16:31], v[72:75], v[212:215], v[16:31]\n"
    "ds_read_b64_tr_b16 v[208:209], v248 offset:12288\n"
    "ds_read_b64_tr_b16 v[210:211], v248 offset:13312\n"
    "s_waitcnt lgkmcnt(12)\n"
    "v_mfma_f32_32x32x16_bf16 v[0:15], v[76:79], v[216:219], v[0:15]\n"
    "ds_read_b64_tr_b16 v[212:213], v248 offset:12800\n"
    "ds_read_b64_tr_b16 v[214:215], v248 offset:13824\n"
    "s_waitcnt lgkmcnt(12)\n"
    "v_mfma_f32_32x32x16_bf16 v[16:31], v[76:79], v[220:223], v[16:31]\n"
    "ds_read_b64_tr_b16 v[216:217], v248 offset:14336\n"
    "ds_read_b64_tr_b16 v[218:219], v248 offset:15360\n"
    "s_waitcnt lgkmcnt(12)\n"
    "v_mfma_f32_32x32x16_bf16 v[0:15], v[80:83], v[192:195], v[0:15]\n"
    "ds_read_b64_tr_b16 v[220:221], v248 offset:14848\n"
    "ds_read_b64_tr_b16 v[222:223], v248 offset:15872\n"
    "s_waitcnt lgkmcnt(12)\n"
    "v_mfma_f32_32x32x16_bf16 v[16:31], v[80:83], v[196:199], v[16:31]\n"
    "s_waitcnt lgkmcnt(10)\n"
    "v_mfma_f32_32x32x16_bf16 v[0:15], v[84:87], v[200:203], v[0:15]\n"
    "s_waitcnt lgkmcnt(8)\n"
    "v_mfma_f32_32x32x16_bf16 v[16:31], v[84:87], v[204:207], v[16:31]\n"
    "s_waitcnt lgkmcnt(6)\n"
    "v_mfma_f32_32x32x16_bf16 v[0:15], v[88:91], v[208:211], v[0:15]\n"
    "s_waitcnt lgkmcnt(4)\n"
    "v_mfma_f32_32x32x16_bf16 v[16:31], v[88:91], v[212:215], v[16:31]\n"
    "s_waitcnt lgkmcnt(2)\n"
    "v_mfma_f32_32x32x16_bf16 v[0:15], v[92:95], v[216:219], v[0:15]\n"
    "s_waitcnt lgkmcnt(0)\n"
    "v_mfma_f32_32x32x16_bf16 v[16:31], v[92:95], v[220:223], v[16:31]\n"
    "s_cmp_eq_u32 s73, 0\n"
    "s_cbranch_scc1 .Lmha_DRAIN\n"
    "s_waitcnt vmcnt(8)\n"
    "s_mov_b64 exec, 1\n"
    "buffer_store_dwordx2 v[242:243], off, s[52:55], s95 sc1\n"
    "s_mov_b64 exec, -1\n"
    "s_cmp_eq_u32 s73, 1\n"
    "s_cbranch_scc1 .Lmha_PATHA2\n"
    "s_branch .Lmha_PATHB2\n"
    ".Lmha_EARLYA:\n"
    "s_mov_b32 s73, 1\n"
    "s_xor_b32 s60, s91, 1\n"
    "s_lshl_b32 s60, s60, 13\n"
    "s_add_u32 s60, s60, s82\n"
    "s_add_u32 s61, s60, 0x1000\n"
    "buffer_load_dwordx4 a[0:3], v239, s[52:55], s60 offen offset:0 sc1\n"
    "buffer_load_dwordx4 a[4:7], v239, s[52:55], s60 offen offset:1024 sc1\n"
    "buffer_load_dwordx4 a[8:11], v239, s[52:55], s60 offen offset:2048 sc1\n"
    "buffer_load_dwordx4 a[12:15], v239, s[52:55], s60 offen offset:3072 sc1\n"
    "buffer_load_dwordx4 a[16:19], v239, s[52:55], s61 offen offset:0 sc1\n"
    "buffer_load_dwordx4 a[20:23], v239, s[52:55], s61 offen offset:1024 sc1\n"
    "buffer_load_dwordx4 a[24:27], v239, s[52:55], s61 offen offset:2048 sc1\n"
    "buffer_load_dwordx4 a[28:31], v239, s[52:55], s61 offen offset:3072 sc1\n"
    "s_branch .Lmha_SLOT0\n"
    ".Lmha_EARLYB:\n"
    "s_mov_b32 s73, 2\n"
    "s_xor_b32 s60, s91, 4\n"
    "s_lshl_b32 s60, s60, 13\n"
    "s_add_u32 s60, s60, s82\n"
    "s_add_u32 s61, s60, 0x1000\n"
    "buffer_load_dwordx4 a[96:99], v239, s[52:55], s60 offen offset:0 sc1\n"
    "buffer_load_dwordx4 a[100:103], v239, s[52:55], s60 offen offset:1024 sc1\n"
    "buffer_load_dwordx4 a[104:107], v239, s[52:55], s60 offen offset:2048 sc1\n"
    "buffer_load_dwordx4 a[108:111], v239, s[52:55], s60 offen offset:3072 sc1\n"
    "buffer_load_dwordx4 a[112:115], v239, s[52:55], s61 offen offset:0 sc1\n"
    "buffer_load_dwordx4 a[116:119], v239, s[52:55], s61 offen offset:1024 sc1\n"
    "buffer_load_dwordx4 a[120:123], v239, s[52:55], s61 offen offset:2048 sc1\n"
    "buffer_load_dwordx4 a[124:127], v239, s[52:55], s61 offen offset:3072 sc1\n"
    "s_branch .Lmha_SLOT0\n"
    ".Lmha_DRAIN:\n"
    "s_waitcnt vmcnt(0)\n"
    "s_mov_b64 exec, 1\n"
    "buffer_store_dwordx2 v[242:243], off, s[52:55], s95 sc1\n"
    "s_mov_b64 exec, -1\n"
    ".Lmha_DEC:\n"
    "s_and_b32 s60, s63, 7\n"
    "s_cbranch_scc0 .Lmha_PATHA\n"
    "s_and_b32 s60, s63, 0x78\n"
    "s_cbranch_scc0 .Lmha_PATHB\n"
    "buffer_load_dwordx2 v[254:255], v244, s[52:55], s88 offen sc1\n"
    "s_sub_u32 s79, s79, 1\n"
    "s_max_i32 s79, s79, 0\n"
    "s_waitcnt vmcnt(0)\n"
    "s_cbranch_scc0 .Lmha_PATHA\n"
    "v_cmp_ne_u64 vcc, s[80:81], v[254:255]\n"
    "s_mov_b32 s63, vcc_lo\n"
    "s_and_b32 s60, s63, 7\n"
    "s_cbranch_scc0 .Lmha_PATHA\n"
    "s_and_b32 s60, s63, 0x78\n"
    "s_cbranch_scc0 .Lmha_PATHB\n"
    "s_add_u32 s18, s18, 1\n"
    "s_cmp_le_u32 s18, 3\n"
    "s_cbranch_scc1 .Lmha_DEC\n"
    "s_sleep 16\n"
    "s_branch .Lmha_DEC\n"
    ".Lmha_PATHB:\n"
    "s_bitcmp1_b32 s63, 3\n"
    "s_cbranch_scc1 .Lmha_FWB4S\n"
    ".Lmha_FWB4:\n"
    "s_xor_b32 s60, s91, 4\n"
    "s_lshl_b32 s60, s60, 13\n"
    "s_add_u32 s60, s60, s82\n"
    "s_add_u32 s61, s60, 0x1000\n"
    "buffer_load_dwordx4 a[96:99], v239, s[52:55], s60 offen offset:0 sc1\n"
    "buffer_load_dwordx4 a[100:103], v239, s[52:55], s60 offen offset:1024 sc1\n"
    "buffer_load_dwordx4 a[104:107], v239, s[52:55], s60 offen offset:2048 sc1\n"
    "buffer_load_dwordx4 a[108:111], v239, s[52:55], s60 offen offset:3072 sc1\n"
    "buffer_load_dwordx4 a[112:115], v239, s[52:55], s61 offen offset:0 sc1\n"
    "buffer_load_dwordx4 a[116:119], v239, s[52:55], s61 offen offset:1024 sc1\n"
    "buffer_load_dwordx4 a[120:123], v239, s[52:55], s61 offen offset:2048 sc1\n"
    "buffer_load_dwordx4 a[124:127], v239, s[52:55], s61 offen offset:3072 sc1\n"
    "buffer_load_dword a255, off, s[52:55], 0 sc1\n"
    ".Lmha_PATHB2:\n"
    "s_bitcmp1_b32 s63, 4\n"
    "s_cbranch_scc1 .Lmha_FWB5S\n"
    ".Lmha_FWB5:\n"
    "s_xor_b32 s60, s91, 5\n"
    "s_lshl_b32 s60, s60, 13\n"
    "s_add_u32 s60, s60, s82\n"
    "s_add_u32 s61, s60, 0x1000\n"
    "buffer_load_dwordx4 a[128:131], v239, s[52:55], s60 offen offset:0 sc1\n"
    "buffer_load_dwordx4 a[132:135], v239, s[52:55], s60 offen offset:1024 sc1\n"
    "buffer_load_dwordx4 a[136:139], v239, s[52:55], s60 offen offset:2048 sc1\n"
    "buffer_load_dwordx4 a[140:143], v239, s[52:55], s60 offen offset:3072 sc1\n"
    "buffer_load_dwordx4 a[144:147], v239, s[52:55], s61 offen offset:0 sc1\n"
    "buffer_load_dwordx4 a[148:151], v239, s[52:55], s61 offen offset:1024 sc1\n"
    "buffer_load_dwordx4 a[152:155], v239, s[52:55], s61 offen offset:2048 sc1\n"
    "buffer_load_dwordx4 a[156:159], v239, s[52:55], s61 offen offset:3072 sc1\n"
    "ds_read_b64_tr_b16 v[192:193], v241 offset:16384\n"
    "ds_read_b64_tr_b16 v[194:195], v241 offset:17408\n"
    "ds_read_b64_tr_b16 v[196:197], v241 offset:16896\n"
    "ds_read_b64_tr_b16 v[198:199], v241 offset:17920\n"
    "ds_read_b64_tr_b16 v[200:201], v241 offset:18432\n"
    "ds_read_b64_tr_b16 v[202:203], v241 offset:19456\n"
    "ds_read_b64_tr_b16 v[204:205], v241 offset:18944\n"
    "ds_read_b64_tr_b16 v[206:207], v241 offset:19968\n"
    "ds_read_b64_tr_b16 v[208:209], v241 offset:20480\n"
    "ds_read_b64_tr_b16 v[210:211], v241 offset:21504\n"
    "ds_read_b64_tr_b16 v[212:213], v241 offset:20992\n"
    "ds_read_b64_tr_b16 v[214:215], v241 offset:22016\n"
    "ds_read_b64_tr_b16 v[216:217], v241 offset:22528\n"
    "ds_read_b64_tr_b16 v[218:219], v241 offset:23552\n"
    "s_bitcmp1_b32 s63, 5\n"
    "s_cbranch_scc1 .Lmha_FWB6S\n"
    ".Lmha_FWB6:\n"
    "s_xor_b32 s60, s91, 6\n"
    "s_lshl_b32 s60, s60, 13\n"
    "s_add_u32 s60, s60, s82\n"
    "s_add_u32 s61, s60, 0x1000\n"
    "s_waitcnt vmcnt(15)\n"
    "s_waitcnt lgkmcnt(12)\n"
    "v_mfma_f32_32x32x16_bf16 v[32:47], a[96:99], v[192:195], 0\n"
    "ds_read_b64_tr_b16 v[220:221], v241 offset:23040\n"
    "ds_read_b64_tr_b16 v[222:223], v241 offset:24064\n"
    "buffer_load_dwordx4 a[160:163], v239, s[52:55], s60 offen offset:0 sc1\n"
    "s_waitcnt lgkmcnt(12)\n"
    "v_mfma_f32_32x32x16_bf16 v[48:63], a[96:99], v[196:199], 0\n"
    "ds_read_b64_tr_b16 v[192:193], v241 offset:24576\n"
    "ds_read_b64_tr_b16 v[194:195], v241 offset:25600\n"
    "s_waitcnt lgkmcnt(12)\n"
    "v_mfma_f32_32x32x16_bf16 v[32:47], a[100:103], v[200:203], v[32:47]\n"
    "ds_read_b64_tr_b16 v[196:197], v241 offset:25088\n"
    "ds_read_b64_tr_b16 v[198:199], v241 offset:26112\n"
    "buffer_load_dwordx4 a[164:167], v239, s[52:55], s60 offen offset:1024 sc1\n"
    "s_waitcnt lgkmcnt(12)\n"
    "v_mfma_f32_32x32x16_bf16 v[48:63], a[100:103], v[204:207], v[48:63]\n"
    "ds_read_b64_tr_b16 v[200:201], v241 offset:26624\n"
    "ds_read_b64_tr_b16 v[202:203], v241 offset:27648\n"
    "s_waitcnt vmcnt(15)\n"
    "s_waitcnt lgkmcnt(12)\n"
    "v_mfma_f32_32x32x16_bf16 v[32:47], a[104:107], v[208:211], v[32:47]\n"
    "ds_read_b64_tr_b16 v[204:205], v241 offset:27136\n"
    "ds_read_b64_tr_b16 v[206:207], v241 offset:28160\n"
    "buffer_load_dwordx4 a[168:171], v239, s[52:55], s60 offen offset:2048 sc1\n"
    "s_waitcnt lgkmcnt(12)\n"
    "v_mfma_f32_32x32x16_bf16 v[48:63], a[104:107], v[212:215], v[48:63]\n"
    "ds_read_b64_tr_b16 v[208:209], v241 offset:28672\n"
    "ds_read_b64_tr_b16 v[210:211], v241 offset:29696\n"
    "s_waitcnt lgkmcnt(12)\n"
    "v_mfma_f32_32x32x16_bf16 v[32:47], a[108:111], v[216:219], v[32:47]\n"
    "ds_read_b64_tr_b16 v[212:213], v241 offset:29184\n"
    "ds_read_b64_tr_b16 v[214:215], v241 offset:30208\n"
    "buffer_load_dwordx4 a[172:175], v239, s[52:55], s60 offen offset:3072 sc1\n"
    "s_waitcnt lgkmcnt(12)\n"
    "v_mfma_f32_32x32x16_bf16 v[48:63], a[108:111], v[220:223], v[48:63]\n"
    "ds_read_b64_tr_b16 v[216:217], v241 offset:30720\n"
    "ds_read_b64_tr_b16 v[218:219], v241 offset:31744\n"
    "s_waitcnt vmcnt(15)\n"
    "s_waitcnt lgkmcnt(12)\n"
    "v_mfma_f32_32x32x16_bf16 v[32:47], a[112:115], v[192:195], v[32:47]\n"
    "ds_read_b64_tr_b16 v[220:221], v241 offset:31232\n"
    "ds_read_b64_tr_b16 v[222:223], v241 offset:32256\n"
    "buffer_load_dwordx4 a[176:179], v239, s[52:55], s61 offen offset:0 sc1\n"
    "s_waitcnt lgkmcnt(12)\n"
    "v_mfma_f32_32x32x16_bf16 v[48:63], a[112:115], v[196:199], v[48:63]\n"
    "ds_read_b64_tr_b16 v[192:193], v241 offset:32768\n"
    "ds_read_b64_tr_b16 v[194:195], v241 offset:33792\n"
    "s_waitcnt lgkmcnt(12)\n"
    "v_mfma_f32_32x32x16_bf16 v[32:47], a[116:119], v[200:203], v[32:47]\n"
    "ds_read_b64_tr_b16 v[196:197], v241 offset:33280\n"
    "ds_read_b64_tr_b16 v[198:199], v241 offset:34304\n"
    "buffer_load_dwordx4 a[180:183], v239, s[52:55], s61 offen offset:1024 sc1\n"
    "s_waitcnt lgkmcnt(12)\n"
    "v_mfma_f32_32x32x16_bf16 v[48:63], a[116:119], v[204:207], v[48:63]\n"
    "ds_read_b64_tr_b16 v[200:201], v241 offset:34816\n"
    "ds_read_b64_tr_b16 v[202:203], v241 offset:35840\n"
    "s_waitcnt vmcnt(15)\n"
    "s_waitcnt lgkmcnt(12)\n"
    "v_mfma_f32_32x32x16_bf16 v[32:47], a[120:123], v[208:211], v[32:47]\n"
    "ds_read_b64_tr_b16 v[204:205], v241 offset:35328\n"
    "ds_read_b64_tr_b16 v[206:207], v241 offset:36352\n"
    "buffer_load_dwordx4 a[184:187], v239, s[52:55], s61 offen offset:2048 sc1\n"
    "s_waitcnt lgkmcnt(12)\n"
    "v_mfma_f32_32x32x16_bf16 v[48:63], a[120:123], v[212:215], v[48:63]\n"
    "ds_read_b64_tr_b16 v[208:209], v241 offset:36864\n"
    "ds_read_b64_tr_b16 v[210:211], v241 offset:37888\n"
    "s_waitcnt lgkmcnt(12)\n"
    "v_mfma_f32_32x32x16_bf16 v[32:47], a[124:127], v[216:219], v[32:47]\n"
    "ds_read_b64_tr_b16 v[212:213], v241 offset:37376\n"
    "ds_read_b64_tr_b16 v[214:215], v241 offset:38400\n"
    "buffer_load_dwordx4 a[188:191], v239, s[52:55], s61 offen offset:3072 sc1\n"
    "s_waitcnt lgkmcnt(12)\n"
    "v_mfma_f32_32x32x16_bf16 v[48:63], a[124:127], v[220:223], v[48:63]\n"
    "ds_read_b64_tr_b16 v[216:217], v241 offset:38912\n"
    "ds_read_b64_tr_b16 v[218:219], v241 offset:39936\n"
    "s_bitcmp1_b32 s63, 6\n"
    "s_cbranch_scc1 .Lmha_FWB7S\n"
    ".Lmha_FWB7:\n"
    "s_xor_b32 s60, s91, 7\n"
    "s_lshl_b32 s60, s60, 13\n"
    "s_add_u32 s60, s60, s82\n"
    "s_add_u32 s61, s60, 0x1000\n"
    "s_waitcnt vmcnt(14)\n"
    "s_waitcnt lgkmcnt(12)\n"
    "v_mfma_f32_32x32x16_bf16 v[32:47], a[128:131], v[192:195], v[32:47]\n"
    "ds_read_b64_tr_b16 v[220:221], v241 offset:39424\n"
    "ds_read_b64_tr_b16 v[222:223], v241 offset:40448\n"
    "buffer_load_dwordx4 a[192:195], v239, s[52:55], s60 offen offset:0 sc1\n"
    "s_waitcnt lgkmcnt(12)\n"
    "v_mfma_f32_32x32x16_bf16 v[48:63], a[128:131], v[196:199], v[48:63]\n"
    "ds_read_b64_tr_b16 v[192:193], v241 offset:40960\n"
    "ds_read_b64_tr_b16 v[194:195], v241 offset:41984\n"
    "s_waitcnt lgkmcnt(12)\n"
    "v_mfma_f32_32x32x16_bf16 v[32:47], a[132:135], v[200:203], v[32:47]\n"
    "ds_read_b64_tr_b16 v[196:197], v241 offset:41472\n"
    "ds_read_b64_tr_b16 v[198:199], v241 offset:42496\n"
    "buffer_load_dwordx4 a[196:199], v239, s[52:55], s60 offen offset:1024 sc1\n"
    "s_waitcnt lgkmcnt(12)\n"
    "v_mfma_f32_32x32x16_bf16 v[48:63], a[132:135], v[204:207], v[48:63]\n"
    "ds_read_b64_tr_b16 v[200:201], v241 offset:43008\n"
    "ds_read_b64_tr_b16 v[202:203], v241 offset:44032\n"
    "s_waitcnt vmcnt(14)\n"
    "s_waitcnt lgkmcnt(12)\n"
    "v_mfma_f32_32x32x16_bf16 v[32:47], a[136:139], v[208:211], v[32:47]\n"
    "ds_read_b64_tr_b16 v[204:205], v241 offset:43520\n"
    "ds_read_b64_tr_b16 v[206:207], v241 offset:44544\n"
    "buffer_load_dwordx4 a[200:203], v239, s[52:55], s60 offen offset:2048 sc1\n"
    "s_waitcnt lgkmcnt(12)\n"
    "v_mfma_f32_32x32x16_bf16 v[48:63], a[136:139], v[212:215], v[48:63]\n"
    "ds_read_b64_tr_b16 v[208:209], v241 offset:45056\n"
    "ds_read_b64_tr_b16 v[210:211], v241 offset:46080\n"
    "s_waitcnt lgkmcnt(12)\n"
    "v_mfma_f32_32x32x16_bf16 v[32:47], a[140:143], v[216:219], v[32:47]\n"
    "ds_read_b64_tr_b16 v[212:213], v241 offset:45568\n"
    "ds_read_b64_tr_b16 v[214:215], v241 offset:46592\n"
    "buffer_load_dwordx4 a[204:207], v239, s[52:55], s60 offen offset:3072 sc1\n"
    "s_waitcnt lgkmcnt(12)\n"
    "v_mfma_f32_32x32x16_bf16 v[48:63], a[140:143], v[220:223], v[48:63]\n"
    "ds_read_b64_tr_b16 v[216:217], v241 offset:47104\n"
    "ds_read_b64_tr_b16 v[218:219], v241 offset:48128\n"
    "s_waitcnt vmcnt(14)\n"
    "s_waitcnt lgkmcnt(12)\n"
    "v_mfma_f32_32x32x16_bf16 v[32:47], a[144:147], v[192:195], v[32:47]\n"
    "ds_read_b64_tr_b16 v[220:221], v241 offset:47616\n"
    "ds_read_b64_tr_b16 v[222:223], v241 offset:48640\n"
    "buffer_load_dwordx4 a[208:211], v239, s[52:55], s61 offen offset:0 sc1\n"
    "s_waitcnt lgkmcnt(12)\n"
    "v_mfma_f32_32x32x16_bf16 v[48:63], a[144:147], v[196:199], v[48:63]\n"
    "ds_read_b64_tr_b16 v[192:193], v241 offset:49152\n"
    "ds_read_b64_tr_b16 v[194:195], v241 offset:50176\n"
    "s_waitcnt lgkmcnt(12)\n"
    "v_mfma_f32_32x32x16_bf16 v[32:47], a[148:151], v[200:203], v[32:47]\n"
    "ds_read_b64_tr_b16 v[196:197], v241 offset:49664\n"
    "ds_read_b64_tr_b16 v[198:199], v241 offset:50688\n"
    "buffer_load_dwordx4 a[212:215], v239, s[52:55], s61 offen offset:1024 sc1\n"
    "s_waitcnt lgkmcnt(12)\n"
    "v_mfma_f32_32x32x16_bf16 v[48:63], a[148:151], v[204:207], v[48:63]\n"
    "ds_read_b64_tr_b16 v[200:201], v241 offset:51200\n"
    "ds_read_b64_tr_b16 v[202:203], v241 offset:52224\n"
    "s_waitcnt vmcnt(14)\n"
    "s_waitcnt lgkmcnt(12)\n"
    "v_mfma_f32_32x32x16_bf16 v[32:47], a[152:155], v[208:211], v[32:47]\n"
    "ds_read_b64_tr_b16 v[204:205], v241 offset:51712\n"
    "ds_read_b64_tr_b16 v[206:207], v241 offset:52736\n"
    "buffer_load_dwordx4 a[216:219], v239, s[52:55], s61 offen offset:2048 sc1\n"
    "s_waitcnt lgkmcnt(12)\n"
    "v_mfma_f32_32x32x16_bf16 v[48:63], a[152:155], v[212:215], v[48:63]\n"
    "ds_read_b64_tr_b16 v[208:209], v241 offset:53248\n"
    "ds_read_b64_tr_b16 v[210:211], v241 offset:54272\n"
    "s_waitcnt lgkmcnt(12)\n"
    "v_mfma_f32_32x32x16_bf16 v[32:47], a[156:159], v[216:219], v[32:47]\n"
    "ds_read_b64_tr_b16 v[212:213], v241 offset:53760\n"
    "ds_read_b64_tr_b16 v[214:215], v241 offset:54784\n"
    "buffer_load_dwordx4 a[220:223], v239, s[52:55], s61 offen offset:3072 sc1\n"
    "s_waitcnt lgkmcnt(12)\n"
    "v_mfma_f32_32x32x16_bf16 v[48:63], a[156:159], v[220:223], v[48:63]\n"
    "ds_read_b64_tr_b16 v[216:217], v241 offset:55296\n"
    "ds_read_b64_tr_b16 v[218:219], v241 offset:56320\n"
    "s_bitcmp1_b32 s63, 0\n"
    "s_cbranch_scc1 .Lmha_FWB1S\n"
    ".Lmha_FWB1:\n"
    "s_xor_b32 s60, s91, 1\n"
    "s_lshl_b32 s60, s60, 13\n"
    "s_add_u32 s60, s60, s82\n"
    "s_add_u32 s61, s60, 0x1000\n"
    "s_waitcnt vmcnt(14)\n"
    "s_waitcnt lgkmcnt(12)\n"
    "v_mfma_f32_32x32x16_bf16 v[32:47], a[160:163], v[192:195], v[32:47]\n"
    "ds_read_b64_tr_b16 v[220:221], v241 offset:55808\n"
    "ds_read_b64_tr_b16 v[222:223], v241 offset:56832\n"
    "buffer_load_dwordx4 a[0:3], v239, s[52:55], s60 offen offset:0 sc1\n"
    "s_waitcnt lgkmcnt(12)\n"
    "v_mfma_f32_32x32x16_bf16 v[48:63], a[160:163], v[196:199], v[48:63]\n"
    "ds_read_b64_tr_b16 v[192:193], v241 offset:57344\n"
    "ds_read_b64_tr_b16 v[194:195], v241 offset:58368\n"
    "s_waitcnt lgkmcnt(12)\n"
    "v_mfma_f32_32x32x16_bf16 v[32:47], a[164:167], v[200:203], v[32:47]\n"
    "ds_read_b64_tr_b16 v[196:197], v241 offset:57856\n"
    "ds_read_b64_tr_b16 v[198:199], v241 offset:58880\n"
    "buffer_load_dwordx4 a[4:7], v239, s[52:55], s60 offen offset:1024 sc1\n"
    "s_waitcnt lgkmcnt(12)\n"
    "v_mfma_f32_32x32x16_bf16 v[48:63], a[164:167], v[204:207], v[48:63]\n"
    "ds_read_b64_tr_b16 v[200:201], v241 offset:59392\n"
    "ds_read_b64_tr_b16 v[202:203], v241 offset:60416\n"
    "s_waitcnt vmcnt(14)\n"
    "s_waitcnt lgkmcnt(12)\n"
    "v_mfma_f32_32x32x16_bf16 v[32:47], a[168:171], v[208:211], v[32:47]\n"
    "ds_read_b64_tr_b16 v[204:205], v241 offset:59904\n"
    "ds_read_b64_tr_b16 v[206:207], v241 offset:60928\n"
    "buffer_load_dwordx4 a[8:11], v239, s[52:55], s60 offen offset:2048 sc1\n"
    "s_waitcnt lgkmcnt(12)\n"
    "v_mfma_f32_32x32x16_bf16 v[48:63], a[168:171], v[212:215], v[48:63]\n"
    "ds_read_b64_tr_b16 v[208:209], v241 offset:61440\n"
    "ds_read_b64_tr_b16 v[210:211], v241 offset:62464\n"
    "s_waitcnt lgkmcnt(12)\n"
    "v_mfma_f32_32x32x16_bf16 v[32:47], a[172:175], v[216:219], v[32:47]\n"
    "ds_read_b64_tr_b16 v[212:213], v241 offset:61952\n"
    "ds_read_b64_tr_b16 v[214:215], v241 offset:62976\n"
    "buffer_load_dwordx4 a[12:15], v239, s[52:55], s60 offen offset:3072 sc1\n"
    "s_waitcnt lgkmcnt(12)\n"
    "v_mfma_f32_32x32x16_bf16 v[48:63], a[172:175], v[220:223], v[48:63]\n"
    "ds_read_b64_tr_b16 v[216:217], v241 offset:63488\n"
    "ds_read_b64_tr_b16 v[218:219], v241 offset:64512\n"
    "s_waitcnt vmcnt(14)\n"
    "s_waitcnt lgkmcnt(12)\n"
    "v_mfma_f32_32x32x16_bf16 v[32:47], a[176:179], v[192:195], v[32:47]\n"
    "ds_read_b64_tr_b16 v[220:221], v241 offset:64000\n"
    "ds_read_b64_tr_b16 v[222:223], v241 offset:65024\n"
    "buffer_load_dwordx4 a[16:19], v239, s[52:55], s61 offen offset:0 sc1\n"
    "s_waitcnt lgkmcnt(12)\n"
    "v_mfma_f32_32x32x16_bf16 v[48:63], a[176:179], v[196:199], v[48:63]\n"
    "ds_read_b64_tr_b16 v[192:193], v246 offset:0\n"
    "ds_read_b64_tr_b16 v[194:195], v246 offset:1024\n"
    "s_waitcnt lgkmcnt(12)\n"
    "v_mfma_f32_32x32x16_bf16 v[32:47], a[180:183], v[200:203], v[32:47]\n"
    "ds_read_b64_tr_b16 v[196:197], v246 offset:512\n"
    "ds_read_b64_tr_b16 v[198:199], v246 offset:1536\n"
    "buffer_load_dwordx4 a[20:23], v239, s[52:55], s61 offen offset:1024 sc1\n"
    "s_waitcnt lgkmcnt(12)\n"
    "v_mfma_f32_32x32x16_bf16 v[48:63], a[180:183], v[204:207], v[48:63]\n"
    "ds_read_b64_tr_b16 v[200:201], v246 offset:2048\n"
    "ds_read_b64_tr_b16 v[202:203], v246 offset:3072\n"
    "s_waitcnt vmcnt(14)\n"
    "s_waitcnt lgkmcnt(12)\n"
    "v_mfma_f32_32x32x16_bf16 v[32:47], a[184:187], v[208:211], v[32:47]\n"
    "ds_read_b64_tr_b16 v[204:205], v246 offset:2560\n"
    "ds_read_b64_tr_b16 v[206:207], v246 offset:3584\n"
    "buffer_load_dwordx4 a[24:27], v239, s[52:55], s61 offen offset:2048 sc1\n"
    "s_waitcnt lgkmcnt(12)\n"
    "v_mfma_f32_32x32x16_bf16 v[48:63], a[184:187], v[212:215], v[48:63]\n"
    "ds_read_b64_tr_b16 v[208:209], v246 offset:4096\n"
    "ds_read_b64_tr_b16 v[210:211], v246 offset:5120\n"
    "s_waitcnt lgkmcnt(12)\n"
    "v_mfma_f32_32x32x16_bf16 v[32:47], a[188:191], v[216:219], v[32:47]\n"
    "ds_read_b64_tr_b16 v[212:213], v246 offset:4608\n"
    "ds_read_b64_tr_b16 v[214:215], v246 offset:5632\n"
    "buffer_load_dwordx4 a[28:31], v239, s[52:55], s61 offen offset:3072 sc1\n"
    "s_waitcnt lgkmcnt(12)\n"
    "v_mfma_f32_32x32x16_bf16 v[48:63], a[188:191], v[220:223], v[48:63]\n"
    "ds_read_b64_tr_b16 v[216:217], v246 offset:6144\n"
    "ds_read_b64_tr_b16 v[218:219], v246 offset:7168\n"
    "s_bitcmp1_b32 s63, 1\n"
    "s_cbranch_scc1 .Lmha_FWB2S\n"
    ".Lmha_FWB2:\n"
    "s_xor_b32 s60, s91, 2\n"
    "s_lshl_b32 s60, s60, 13\n"
    "s_add_u32 s60, s60, s82\n"
    "s_add_u32 s61, s60, 0x1000\n"
    "s_waitcnt vmcnt(14)\n"
    "s_waitcnt lgkmcnt(12)\n"
    "v_mfma_f32_32x32x16_bf16 v[32:47], a[192:195], v[192:195], v[32:47]\n"
    "ds_read_b64_tr_b16 v[220:221], v246 offset:6656\n"
    "ds_read_b64_tr_b16 v[222:223], v246 offset:7680\n"
    "buffer_load_dwordx4 a[32:35], v239, s[52:55], s60 offen offset:0 sc1\n"
    "s_waitcnt lgkmcnt(12)\n"
    "v_mfma_f32_32x32x16_bf16 v[48:63], a[192:195], v[196:199], v[48:63]\n"
    "ds_read_b64_tr_b16 v[192:193], v246 offset:8192\n"
    "ds_read_b64_tr_b16 v[194:195], v246 offset:9216\n"
    "s_waitcnt lgkmcnt(12)\n"
    "v_mfma_f32_32x32x16_bf16 v[32:47], a[196:199], v[200:203], v[32:47]\n"
    "ds_read_b64_tr_b16 v[196:197], v246 offset:8704\n"
    "ds_read_b64_tr_b16 v[198:199], v246 offset:9728\n"
    "buffer_load_dwordx4 a[36:39], v239, s[52:55], s60 offen offset:1024 sc1\n"
    "s_waitcnt lgkmcnt(12)\n"
    "v_mfma_f32_32x32x16_bf16 v[48:63], a[196:199], v[204:207], v[48:63]\n"
    "ds_read_b64_tr_b16 v[200:201], v246 offset:10240\n"
    "ds_read_b64_tr_b16 v[202:203], v246 offset:11264\n"
    "s_waitcnt vmcnt(14)\n"
    "s_waitcnt lgkmcnt(12)\n"
    "v_mfma_f32_32x32x16_bf16 v[32:47], a[200:203], v[208:211], v[32:47]\n"
    "ds_read_b64_tr_b16 v[204:205], v246 offset:10752\n"
    "ds_read_b64_tr_b16 v[206:207], v246 offset:11776\n"
    "buffer_load_dwordx4 a[40:43], v239, s[52:55], s60 offen offset:2048 sc1\n"
    "s_waitcnt lgkmcnt(12)\n"
    "v_mfma_f32_32x32x16_bf16 v[48:63], a[200:203], v[212:215], v[48:63]\n"
    "ds_read_b64_tr_b16 v[208:209], v246 offset:12288\n"
    "ds_read_b64_tr_b16 v[210:211], v246 offset:13312\n"
    "s_waitcnt lgkmcnt(12)\n"
    "v_mfma_f32_32x32x16_bf16 v[32:47], a[204:207], v[216:219], v[32:47]\n"
    "ds_read_b64_tr_b16 v[212:213], v246 offset:12800\n"
    "ds_read_b64_tr_b16 v[214:215], v246 offset:13824\n"
    "ds_read_b32 v252, v225\n"
    "buffer_load_dwordx4 a[44:47], v239, s[52:55], s60 offen offset:3072 sc1\n"
    "s_waitcnt lgkmcnt(13)\n"
    "v_mfma_f32_32x32x16_bf16 v[48:63], a[204:207], v[220:223], v[48:63]\n"
    "ds_read_b64_tr_b16 v[216:217], v246 offset:14336\n"
    "ds_read_b64_tr_b16 v[218:219], v246 offset:15360\n"
    "s_waitcnt vmcnt(14)\n"
    "s_waitcnt lgkmcnt(13)\n"
    "v_mfma_f32_32x32x16_bf16 v[32:47], a[208:211], v[192:195], v[32:47]\n"
    "ds_read_b64_tr_b16 v[220:221], v246 offset:14848\n"
    "ds_read_b64_tr_b16 v[222:223], v246 offset:15872\n"
    "buffer_load_dwordx4 a[48:51], v239, s[52:55], s61 offen offset:0 sc1\n"
    "s_waitcnt lgkmcnt(13)\n"
    "v_mfma_f32_32x32x16_bf16 v[48:63], a[208:211], v[196:199], v[48:63]\n"
    "s_waitcnt lgkmcnt(4)\n"
    "v_readfirstlane_b32 s72, v252\n"
    "s_mov_b32 s74, 92\n"
    "s_cmp_ge_u32 s72, s74\n"
    "s_cbranch_scc0 .Lmha_GB4S\n"
    ".Lmha_GB4:\n"
    "ds_read_b64_tr_b16 v[192:193], v246 offset:16384\n"
    "ds_read_b64_tr_b16 v[194:195], v246 offset:17408\n"
    "v_mfma_f32_32x32x16_bf16 v[32:47], a[212:215], v[200:203], v[32:47]\n"
    "ds_read_b64_tr_b16 v[196:197], v246 offset:16896\n"
    "ds_read_b64_tr_b16 v[198:199], v246 offset:17920\n"
    "buffer_load_dwordx4 a[52:55], v239, s[52:55], s61 offen offset:1024 sc1\n"
    "v_mfma_f32_32x32x16_bf16 v[48:63], a[212:215], v[204:207], v[48:63]\n"
    "ds_read_b64_tr_b16 v[200:201], v246 offset:18432\n"
    "ds_read_b64_tr_b16 v[202:203], v246 offset:19456\n"
    "s_waitcnt vmcnt(14)\n"
    "v_mfma_f32_32x32x16_bf16 v[32:47], a[216:219], v[208:211], v[32:47]\n"
    "ds_read_b64_tr_b16 v[204:205], v246 offset:18944\n"
    "ds_read_b64_tr_b16 v[206:207], v246 offset:19968\n"
    "buffer_load_dwordx4 a[56:59], v239, s[52:55], s61 offen offset:2048 sc1\n"
    "v_mfma_f32_32x32x16_bf16 v[48:63], a[216:219], v[212:215], v[48:63]\n"
    "ds_read_b64_tr_b16 v[208:209], v246 offset:20480\n"
    "ds_read_b64_tr_b16 v[210:211], v246 offset:21504\n"
    "s_waitcnt lgkmcnt(12)\n"
    "v_mfma_f32_32x32x16_bf16 v[32:47], a[220:223], v[216:219], v[32:47]\n"
    "ds_read_b64_tr_b16 v[212:213], v246 offset:20992\n"
    "ds_read_b64_tr_b16 v[214:215], v246 offset:22016\n"
    "buffer_load_dwordx4 a[60:63], v239, s[52:55], s61 offen offset:3072 sc1\n"
    "s_waitcnt lgkmcnt(12)\n"
    "v_mfma_f32_32x32x16_bf16 v[48:63], a[220:223], v[220:223], v[48:63]\n"
    "ds_read_b64_tr_b16 v[216:217], v246 offset:22528\n"
    "ds_read_b64_tr_b16 v[218:219], v246 offset:23552\n"
    "s_bitcmp1_b32 s63, 2\n"
    "s_cbranch_scc1 .Lmha_FWB3S\n"
    ".Lmha_FWB3:\n"
    "s_xor_b32 s60, s91, 3\n"
    "s_lshl_b32 s60, s60, 13\n"
    "s_add_u32 s60, s60, s82\n"
    "s_add_u32 s61, s60, 0x1000\n"
    "s_waitcnt vmcnt(14)\n"
    "s_waitcnt lgkmcnt(12)\n"
    "v_mfma_f32_32x32x16_bf16 v[0:15], a[0:3], v[192:195], v[0:15]\n"
    "ds_read_b64_tr_b16 v[220:221], v246 offset:23040\n"
    "ds_read_b64_tr_b16 v[222:223], v246 offset:24064\n"
    "buffer_load_dwordx4 a[64:67], v239, s[52:55], s60 offen offset:0 sc1\n"
    "s_waitcnt lgkmcnt(12)\n"
    "v_mfma_f32_32x32x16_bf16 v[16:31], a[0:3], v[196:199], v[16:31]\n"
    "ds_read_b64_tr_b16 v[192:193], v246 offset:24576\n"
    "ds_read_b64_tr_b16 v[194:195], v246 offset:25600\n"
    "s_waitcnt lgkmcnt(12)\n"
    "v_mfma_f32_32x32x16_bf16 v[0:15], a[4:7], v[200:203], v[0:15]\n"
    "ds_read_b64_tr_b16 v[196:197], v246 offset:25088\n"
    "ds_read_b64_tr_b16 v[198:199], v246 offset:26112\n"
    "buffer_load_dwordx4 a[68:71], v239, s[52:55], s60 offen offset:1024 sc1\n"
    "s_waitcnt lgkmcnt(12)\n"
    "v_mfma_f32_32x32x16_bf16 v[16:31], a[4:7], v[204:207], v[16:31]\n"
    "ds_read_b64_tr_b16 v[200:201], v246 offset:26624\n"
    "ds_read_b64_tr_b16 v[202:203], v246 offset:27648\n"
    "s_waitcnt vmcnt(14)\n"
    "s_waitcnt lgkmcnt(12)\n"
    "v_mfma_f32_32x32x16_bf16 v[0:15], a[8:11], v[208:211], v[0:15]\n"
    "ds_read_b64_tr_b16 v[204:205], v246 offset:27136\n"
    "ds_read_b64_tr_b16 v[206:207], v246 offset:28160\n"
    "buffer_load_dwordx4 a[72:75], v239, s[52:55], s60 offen offset:2048 sc1\n"
    "s_waitcnt lgkmcnt(12)\n"
    "v_mfma_f32_32x32x16_bf16 v[16:31], a[8:11], v[212:215], v[16:31]\n"
    "ds_read_b64_tr_b16 v[208:209], v246 offset:28672\n"
    "ds_read_b64_tr_b16 v[210:211], v246 offset:29696\n"
    "s_waitcnt lgkmcnt(12)\n"
    "v_mfma_f32_32x32x16_bf16 v[0:15], a[12:15], v[216:219], v[0:15]\n"
    "ds_read_b64_tr_b16 v[212:213], v246 offset:29184\n"
    "ds_read_b64_tr_b16 v[214:215], v246 offset:30208\n"
    "ds_read_b32 v252, v225\n"
    "buffer_load_dwordx4 a[76:79], v239, s[52:55], s60 offen offset:3072 sc1\n"
    "s_waitcnt lgkmcnt(13)\n"
    "v_mfma_f32_32x32x16_bf16 v[16:31], a[12:15], v[220:223], v[16:31]\n"
    "ds_read_b64_tr_b16 v[216:217], v246 offset:30720\n"
    "ds_read_b64_tr_b16 v[218:219], v246 offset:31744\n"
    "s_waitcnt vmcnt(14)\n"
    "s_waitcnt lgkmcnt(13)\n"
    "v_mfma_f32_32x32x16_bf16 v[0:15], a[16:19], v[192:195], v[0:15]\n"
    "ds_read_b64_tr_b16 v[220:221], v246 offset:31232\n"
    "ds_read_b64_tr_b16 v[222:223], v246 offset:32256\n"
    "buffer_load_dwordx4 a[80:83], v239, s[52:55], s61 offen offset:0 sc1\n"
    "s_waitcnt lgkmcnt(13)\n"
    "v_mfma_f32_32x32x16_bf16 v[16:31], a[16:19], v[196:199], v[16:31]\n"
    "s_waitcnt lgkmcnt(4)\n"
    "v_readfirstlane_b32 s72, v252\n"
    "s_mov_b32 s74, 94\n"
    "s_cmp_ge_u32 s72, s74\n"
    "s_cbranch_scc0 .Lmha_GB5S\n"
    ".Lmha_GB5:\n"
    "ds_read_b64_tr_b16 v[192:193], v246 offset:32768\n"
    "ds_read_b64_tr_b16 v[194:195], v246 offset:33792\n"
    "v_mfma_f32_32x32x16_bf16 v[0:15], a[20:23], v[200:203], v[0:15]\n"
    "ds_read_b64_tr_b16 v[196:197], v246 offset:33280\n"
    "ds_read_b64_tr_b16 v[198:199], v246 offset:34304\n"
    "buffer_load_dwordx4 a[84:87], v239, s[52:55], s61 offen offset:1024 sc1\n"
    "v_mfma_f32_32x32x16_bf16 v[16:31], a[20:23], v[204:207], v[16:31]\n"
    "ds_read_b64_tr_b16 v[200:201], v246 offset:34816\n"
    "ds_read_b64_tr_b16 v[202:203], v246 offset:35840\n"
    "s_waitcnt vmcnt(14)\n"
    "v_mfma_f32_32x32x16_bf16 v[0:15], a[24:27], v[208:211], v[0:15]\n"
    "ds_read_b64_tr_b16 v[204:205], v246 offset:35328\n"
    "ds_read_b64_tr_b16 v[206:207], v246 offset:36352\n"
    "buffer_load_dwordx4 a[88:91], v239, s[52:55], s61 offen offset:2048 sc1\n"
    "v_mfma_f32_32x32x16_bf16 v[16:31], a[24:27], v[212:215], v[16:31]\n"
    "ds_read_b64_tr_b16 v[208:209], v246 offset:36864\n"
    "ds_read_b64_tr_b16 v[210:211], v246 offset:37888\n"
    "s_waitcnt lgkmcnt(12)\n"
    "v_mfma_f32_32x32x16_bf16 v[0:15], a[28:31], v[216:219], v[0:15]\n"
    "ds_read_b64_tr_b16 v[212:213], v246 offset:37376\n"
    "ds_read_b64_tr_b16 v[214:215], v246 offset:38400\n"
    "buffer_load_dwordx4 a[92:95], v239, s[52:55], s61 offen offset:3072 sc1\n"
    "s_waitcnt lgkmcnt(12)\n"
    "v_mfma_f32_32x32x16_bf16 v[16:31], a[28:31], v[220:223], v[16:31]\n"
    "ds_read_b64_tr_b16 v[216:217], v246 offset:38912\n"
    "ds_read_b64_tr_b16 v[218:219], v246 offset:39936\n"
    "s_waitcnt vmcnt(14)\n"
    "s_waitcnt lgkmcnt(12)\n"
    "v_mfma_f32_32x32x16_bf16 v[0:15], a[32:35], v[192:195], v[0:15]\n"
    "ds_read_b64_tr_b16 v[220:221], v246 offset:39424\n"
    "ds_read_b64_tr_b16 v[222:223], v246 offset:40448\n"
    "s_waitcnt lgkmcnt(12)\n"
    "v_mfma_f32_32x32x16_bf16 v[16:31], a[32:35], v[196:199], v[16:31]\n"
    "ds_read_b64_tr_b16 v[192:193], v246 offset:40960\n"
    "ds_read_b64_tr_b16 v[194:195], v246 offset:41984\n"
    "s_waitcnt lgkmcnt(12)\n"
    "v_mfma_f32_32x32x16_bf16 v[0:15], a[36:39], v[200:203], v[0:15]\n"
    "ds_read_b64_tr_b16 v[196:197], v246 offset:41472\n"
    "ds_read_b64_tr_b16 v[198:199], v246 offset:42496\n"
    "s_waitcnt lgkmcnt(12)\n"
    "v_mfma_f32_32x32x16_bf16 v[16:31], a[36:39], v[204:207], v[16:31]\n"
    "ds_read_b64_tr_b16 v[200:201], v246 offset:43008\n"
    "ds_read_b64_tr_b16 v[202:203], v246 offset:44032\n"
    "s_waitcnt vmcnt(12)\n"
    "s_waitcnt lgkmcnt(12)\n"
    "v_mfma_f32_32x32x16_bf16 v[0:15], a[40:43], v[208:211], v[0:15]\n"
    "ds_read_b64_tr_b16 v[204:205], v246 offset:43520\n"
    "ds_read_b64_tr_b16 v[206:207], v246 offset:44544\n"
    "s_waitcnt lgkmcnt(12)\n"
    "v_mfma_f32_32x32x16_bf16 v[16:31], a[40:43], v[212:215], v[16:31]\n"
    "ds_read_b64_tr_b16 v[208:209], v246 offset:45056\n"
    "ds_read_b64_tr_b16 v[210:211], v246 offset:46080\n"
    "s_waitcnt lgkmcnt(12)\n"
    "v_mfma_f32_32x32x16_bf16 v[0:15], a[44:47], v[216:219], v[0:15]\n"
    "ds_read_b64_tr_b16 v[212:213], v246 offset:45568\n"
    "ds_read_b64_tr_b16 v[214:215], v246 offset:46592\n"
    "ds_read_b32 v252, v225\n"
    "s_waitcnt lgkmcnt(13)\n"
    "v_mfma_f32_32x32x16_bf16 v[16:31], a[44:47], v[220:223], v[16:31]\n"
    "ds_read_b64_tr_b16 v[216:217], v246 offset:47104\n"
    "ds_read_b64_tr_b16 v[218:219], v246 offset:48128\n"
    "s_waitcnt vmcnt(10)\n"
    "s_waitcnt lgkmcnt(13)\n"
    "v_mfma_f32_32x32x16_bf16 v[0:15], a[48:51], v[192:195], v[0:15]\n"
    "ds_read_b64_tr_b16 v[220:221], v246 offset:47616\n"
    "ds_read_b64_tr_b16 v[222:223], v246 offset:48640\n"
    "s_waitcnt lgkmcnt(13)\n"
    "v_mfma_f32_32x32x16_bf16 v[16:31], a[48:51], v[196:199], v[16:31]\n"
    "s_waitcnt lgkmcnt(4)\n"
    "v_readfirstlane_b32 s72, v252\n"
    "s_mov_b32 s74, 96\n"
    "s_cmp_ge_u32 s72, s74\n"
    "s_cbranch_scc0 .Lmha_GB6S\n"
    ".Lmha_GB6:\n"
    "ds_read_b64_tr_b16 v[192:193], v246 offset:49152\n"
    "ds_read_b64_tr_b16 v[194:195], v246 offset:50176\n"
    "v_mfma_f32_32x32x16_bf16 v[0:15], a[52:55], v[200:203], v[0:15]\n"
    "ds_read_b64_tr_b16 v[196:197], v246 offset:49664\n"
    "ds_read_b64_tr_b16 v[198:199], v246 offset:50688\n"
    "v_mfma_f32_32x32x16_bf16 v[16:31], a[52:55], v[204:207], v[16:31]\n"
    "ds_read_b64_tr_b16 v[200:201], v246 offset:51200\n"
    "ds_read_b64_tr_b16 v[202:203], v246 offset:52224\n"
    "s_waitcnt vmcnt(8)\n"
    "v_mfma_f32_32x32x16_bf16 v[0:15], a[56:59], v[208:211], v[0:15]\n"
    "ds_read_b64_tr_b16 v[204:205], v246 offset:51712\n"
    "ds_read_b64_tr_b16 v[206:207], v246 offset:52736\n"
    "v_mfma_f32_32x32x16_bf16 v[16:31], a[56:59], v[212:215], v[16:31]\n"
    "ds_read_b64_tr_b16 v[208:209], v246 offset:53248\n"
    "ds_read_b64_tr_b16 v[210:211], v246 offset:54272\n"
    "s_waitcnt lgkmcnt(12)\n"
    "v_mfma_f32_32x32x16_bf16 v[0:15], a[60:63], v[216:219], v[0:15]\n"
    "ds_read_b64_tr_b16 v[212:213], v246 offset:53760\n"
    "ds_read_b64_tr_b16 v[214:215], v246 offset:54784\n"
    "s_waitcnt lgkmcnt(12)\n"
    "v_mfma_f32_32x32x16_bf16 v[16:31], a[60:63], v[220:223], v[16:31]\n"
    "ds_read_b64_tr_b16 v[216:217], v246 offset:55296\n"
    "ds_read_b64_tr_b16 v[218:219], v246 offset:56320\n"
    "s_waitcnt vmcnt(6)\n"
    "s_waitcnt lgkmcnt(12)\n"
    "v_mfma_f32_32x32x16_bf16 v[0:15], a[64:67], v[192:195], v[0:15]\n"
    "ds_read_b64_tr_b16 v[220:221], v246 offset:55808\n"
    "ds_read_b64_tr_b16 v[222:223], v246 offset:56832\n"
    "s_waitcnt lgkmcnt(12)\n"
    "v_mfma_f32_32x32x16_bf16 v[16:31], a[64:67], v[196:199], v[16:31]\n"
    "ds_read_b64_tr_b16 v[192:193], v246 offset:57344\n"
    "ds_read_b64_tr_b16 v[194:195], v246 offset:58368\n"
    "s_waitcnt lgkmcnt(12)\n"
    "v_mfma_f32_32x32x16_bf16 v[0:15], a[68:71], v[200:203], v[0:15]\n"
    "ds_read_b64_tr_b16 v[196:197], v246 offset:57856\n"
    "ds_read_b64_tr_b16 v[198:199], v246 offset:58880\n"
    "s_waitcnt lgkmcnt(12)\n"
    "v_mfma_f32_32x32x16_bf16 v[16:31], a[68:71], v[204:207], v[16:31]\n"
    "ds_read_b64_tr_b16 v[200:201], v246 offset:59392\n"
    "ds_read_b64_tr_b16 v[202:203], v246 offset:60416\n"
    "s_waitcnt vmcnt(4)\n"
    "s_waitcnt lgkmcnt(12)\n"
    "v_mfma_f32_32x32x16_bf16 v[0:15], a[72:75], v[208:211], v[0:15]\n"
    "ds_read_b64_tr_b16 v[204:205], v246 offset:59904\n"
    "ds_read_b64_tr_b16 v[206:207], v246 offset:60928\n"
    "s_waitcnt lgkmcnt(12)\n"
    "v_mfma_f32_32x32x16_bf16 v[16:31], a[72:75], v[212:215], v[16:31]\n"
    "ds_read_b64_tr_b16 v[208:209], v246 offset:61440\n"
    "ds_read_b64_tr_b16 v[210:211], v246 offset:62464\n"
    "s_waitcnt lgkmcnt(12)\n"
    "v_mfma_f32_32x32x16_bf16 v[0:15], a[76:79], v[216:219], v[0:15]\n"
    "ds_read_b64_tr_b16 v[212:213], v246 offset:61952\n"
    "ds_read_b64_tr_b16 v[214:215], v246 offset:62976\n"
    "s_waitcnt lgkmcnt(12)\n"
    "v_mfma_f32_32x32x16_bf16 v[16:31], a[76:79], v[220:223], v[16:31]\n"
    "ds_read_b64_tr_b16 v[216:217], v246 offset:63488\n"
    "ds_read_b64_tr_b16 v[218:219], v246 offset:64512\n"
    "s_waitcnt vmcnt(2)\n"
    "s_waitcnt lgkmcnt(12)\n"
    "v_mfma_f32_32x32x16_bf16 v[0:15], a[80:83], v[192:195], v[0:15]\n"
    "ds_read_b64_tr_b16 v[220:221], v246 offset:64000\n"
    "ds_read_b64_tr_b16 v[222:223], v246 offset:65024\n"
    "s_waitcnt lgkmcnt(12)\n"
    "v_mfma_f32_32x32x16_bf16 v[16:31], a[80:83], v[196:199], v[16:31]\n"
    "s_waitcnt lgkmcnt(10)\n"
    "v_mfma_f32_32x32x16_bf16 v[0:15], a[84:87], v[200:203], v[0:15]\n"
    "s_waitcnt lgkmcnt(8)\n"
    "v_mfma_f32_32x32x16_bf16 v[16:31], a[84:87], v[204:207], v[16:31]\n"
    "s_waitcnt vmcnt(0)\n"
    "s_waitcnt lgkmcnt(6)\n"
    "v_mfma_f32_32x32x16_bf16 v[0:15], a[88:91], v[208:211], v[0:15]\n"
    "s_waitcnt lgkmcnt(4)\n"
    "v_mfma_f32_32x32x16_bf16 v[16:31], a[88:91], v[212:215], v[16:31]\n"
    "s_waitcnt lgkmcnt(2)\n"
    "v_mfma_f32_32x32x16_bf16 v[0:15], a[92:95], v[216:219], v[0:15]\n"
    "s_waitcnt lgkmcnt(0)\n"
    "v_mfma_f32_32x32x16_bf16 v[16:31], a[92:95], v[220:223], v[16:31]\n"
    "s_branch .Lmha_GDONE\n"
    ".Lmha_PATHA:\n"
    "s_bitcmp1_b32 s63, 0\n"
    "s_cbranch_scc1 .Lmha_FWA1S\n"
    ".Lmha_FWA1:\n"
    "s_xor_b32 s60, s91, 1\n"
    "s_lshl_b32 s60, s60, 13\n"
    "s_add_u32 s60, s60, s82\n"
    "s_add_u32 s61, s60, 0x1000\n"
    "buffer_load_dwordx4 a[0:3], v239, s[52:55], s60 offen offset:0 sc1\n"
    "buffer_load_dwordx4 a[4:7], v239, s[52:55], s60 offen offset:1024 sc1\n"
    "buffer_load_dwordx4 a[8:11], v239, s[52:55], s60 offen offset:2048 sc1\n"
    "buffer_load_dwordx4 a[12:15], v239, s[52:55], s60 offen offset:3072 sc1\n"
    "buffer_load_dwordx4 a[16:19], v239, s[52:55], s61 offen offset:0 sc1\n"
    "buffer_load_dwordx4 a[20:23], v239, s[52:55], s61 offen offset:1024 sc1\n"
    "buffer_load_dwordx4 a[24:27], v239, s[52:55], s61 offen offset:2048 sc1\n"
    "buffer_load_dwordx4 a[28:31], v239, s[52:55], s61 offen offset:3072 sc1\n"
    "buffer_load_dword a255, off, s[52:55], 0 sc1\n"
    ".Lmha_PATHA2:\n"
    "s_bitcmp1_b32 s63, 1\n"
    "s_cbranch_scc1 .Lmha_FWA2S\n"
    ".Lmha_FWA2:\n"
    "s_xor_b32 s60, s91, 2\n"
    "s_lshl_b32 s60, s60, 13\n"
    "s_add_u32 s60, s60, s82\n"
    "s_add_u32 s61, s60, 0x1000\n"
    "buffer_load_dwordx4 a[32:35], v239, s[52:55], s60 offen offset:0 sc1\n"
    "buffer_load_dwordx4 a[36:39], v239, s[52:55], s60 offen offset:1024 sc1\n"
    "buffer_load_dwordx4 a[40:43], v239, s[52:55], s60 offen offset:2048 sc1\n"
    "buffer_load_dwordx4 a[44:47], v239, s[52:55], s60 offen offset:3072 sc1\n"
    "buffer_load_dwordx4 a[48:51], v239, s[52:55], s61 offen offset:0 sc1\n"
    "buffer_load_dwordx4 a[52:55], v239, s[52:55], s61 offen offset:1024 sc1\n"
    "buffer_load_dwordx4 a[56:59], v239, s[52:55], s61 offen offset:2048 sc1\n"
    "buffer_load_dwordx4 a[60:63], v239, s[52:55], s61 offen offset:3072 sc1\n"
    "s_mov_b32 s74, 92\n"
    "s_cmp_ge_u32 s72, s74\n"
    "s_cbranch_scc0 .Lmha_GA0S\n"
    ".Lmha_GA0:\n"
    "ds_read_b64_tr_b16 v[192:193], v246 offset:16384\n"
    "ds_read_b64_tr_b16 v[194:195], v246 offset:17408\n"
    "ds_read_b64_tr_b16 v[196:197], v246 offset:16896\n"
    "ds_read_b64_tr_b16 v[198:199], v246 offset:17920\n"
    "ds_read_b64_tr_b16 v[200:201], v246 offset:18432\n"
    "ds_read_b64_tr_b16 v[202:203], v246 offset:19456\n"
    "ds_read_b64_tr_b16 v[204:205], v246 offset:18944\n"
    "ds_read_b64_tr_b16 v[206:207], v246 offset:19968\n"
    "ds_read_b64_tr_b16 v[208:209], v246 offset:20480\n"
    "ds_read_b64_tr_b16 v[210:211], v246 offset:21504\n"
    "ds_read_b64_tr_b16 v[212:213], v246 offset:20992\n"
    "ds_read_b64_tr_b16 v[214:215], v246 offset:22016\n"
    "ds_read_b64_tr_b16 v[216:217], v246 offset:22528\n"
    "ds_read_b64_tr_b16 v[218:219], v246 offset:23552\n"
    "s_bitcmp1_b32 s63, 2\n"
    "s_cbranch_scc1 .Lmha_FWA3S\n"
    ".Lmha_FWA3:\n"
    "s_xor_b32 s60, s91, 3\n"
    "s_lshl_b32 s60, s60, 13\n"
    "s_add_u32 s60, s60, s82\n"
    "s_add_u32 s61, s60, 0x1000\n"
    "s_waitcnt vmcnt(15)\n"
    "s_waitcnt lgkmcnt(12)\n"
    "v_mfma_f32_32x32x16_bf16 v[0:15], a[0:3], v[192:195], v[0:15]\n"
    "ds_read_b64_tr_b16 v[220:221], v246 offset:23040\n"
    "ds_read_b64_tr_b16 v[222:223], v246 offset:24064\n"
    "buffer_load_dwordx4 a[64:67], v239, s[52:55], s60 offen offset:0 sc1\n"
    "s_waitcnt lgkmcnt(12)\n"
    "v_mfma_f32_32x32x16_bf16 v[16:31], a[0:3], v[196:199], v[16:31]\n"
    "ds_read_b64_tr_b16 v[192:193], v246 offset:24576\n"
    "ds_read_b64_tr_b16 v[194:195], v246 offset:25600\n"
    "s_waitcnt lgkmcnt(12)\n"
    "v_mfma_f32_32x32x16_bf16 v[0:15], a[4:7], v[200:203], v[0:15]\n"
    "ds_read_b64_tr_b16 v[196:197], v246 offset:25088\n"
    "ds_read_b64_tr_b16 v[198:199], v246 offset:26112\n"
    "buffer_load_dwordx4 a[68:71], v239, s[52:55], s60 offen offset:1024 sc1\n"
    "s_waitcnt lgkmcnt(12)\n"
    "v_mfma_f32_32x32x16_bf16 v[16:31], a[4:7], v[204:207], v[16:31]\n"
    "ds_read_b64_tr_b16 v[200:201], v246 offset:26624\n"
    "ds_read_b64_tr_b16 v[202:203], v246 offset:27648\n"
    "s_waitcnt vmcnt(15)\n"
    "s_waitcnt lgkmcnt(12)\n"
    "v_mfma_f32_32x32x16_bf16 v[0:15], a[8:11], v[208:211], v[0:15]\n"
    "ds_read_b64_tr_b16 v[204:205], v246 offset:27136\n"
    "ds_read_b64_tr_b16 v[206:207], v246 offset:28160\n"
    "buffer_load_dwordx4 a[72:75], v239, s[52:55], s60 offen offset:2048 sc1\n"
    "s_waitcnt lgkmcnt(12)\n"
    "v_mfma_f32_32x32x16_bf16 v[16:31], a[8:11], v[212:215], v[16:31]\n"
    "ds_read_b64_tr_b16 v[208:209], v246 offset:28672\n"
    "ds_read_b64_tr_b16 v[210:211], v246 offset:29696\n"
    "s_waitcnt lgkmcnt(12)\n"
    "v_mfma_f32_32x32x16_bf16 v[0:15], a[12:15], v[216:219], v[0:15]\n"
    "ds_read_b64_tr_b16 v[212:213], v246 offset:29184\n"
    "ds_read_b64_tr_b16 v[214:215], v246 offset:30208\n"
    "ds_read_b32 v252, v225\n"
    "buffer_load_dwordx4 a[76:79], v239, s[52:55], s60 offen offset:3072 sc1\n"
    "s_waitcnt lgkmcnt(13)\n"
    "v_mfma_f32_32x32x16_bf16 v[16:31], a[12:15], v[220:223], v[16:31]\n"
    "ds_read_b64_tr_b16 v[216:217], v246 offset:30720\n"
    "ds_read_b64_tr_b16 v[218:219], v246 offset:31744\n"
    "s_waitcnt vmcnt(15)\n"
    "s_waitcnt lgkmcnt(13)\n"
    "v_mfma_f32_32x32x16_bf16 v[0:15], a[16:19], v[192:195], v[0:15]\n"
    "ds_read_b64_tr_b16 v[220:221], v246 offset:31232\n"
    "ds_read_b64_tr_b16 v[222:223], v246 offset:32256\n"
    "buffer_load_dwordx4 a[80:83], v239, s[52:55], s61 offen offset:0 sc1\n"
    "s_waitcnt lgkmcnt(13)\n"
    "v_mfma_f32_32x32x16_bf16 v[16:31], a[16:19], v[196:199], v[16:31]\n"
    "s_waitcnt lgkmcnt(4)\n"
    "v_readfirstlane_b32 s72, v252\n"
    "s_mov_b32 s74, 94\n"
    "s_cmp_ge_u32 s72, s74\n"
    "s_cbranch_scc0 .Lmha_GA1S\n"
    ".Lmha_GA1:\n"
    "ds_read_b64_tr_b16 v[192:193], v246 offset:32768\n"
    "ds_read_b64_tr_b16 v[194:195], v246 offset:33792\n"
    "v_mfma_f32_32x32x16_bf16 v[0:15], a[20:23], v[200:203], v[0:15]\n"
    "ds_read_b64_tr_b16 v[196:197], v246 offset:33280\n"
    "ds_read_b64_tr_b16 v[198:199], v246 offset:34304\n"
    "buffer_load_dwordx4 a[84:87], v239, s[52:55], s61 offen offset:1024 sc1\n"
    "v_mfma_f32_32x32x16_bf16 v[16:31], a[20:23], v[204:207], v[16:31]\n"
    "ds_read_b64_tr_b16 v[200:201], v246 offset:34816\n"
    "ds_read_b64_tr_b16 v[202:203], v246 offset:35840\n"
    "s_waitcnt vmcnt(15)\n"
    "v_mfma_f32_32x32x16_bf16 v[0:15], a[24:27], v[208:211], v[0:15]\n"
    "ds_read_b64_tr_b16 v[204:205], v246 offset:35328\n"
    "ds_read_b64_tr_b16 v[206:207], v246 offset:36352\n"
    "buffer_load_dwordx4 a[88:91], v239, s[52:55], s61 offen offset:2048 sc1\n"
    "v_mfma_f32_32x32x16_bf16 v[16:31], a[24:27], v[212:215], v[16:31]\n"
    "ds_read_b64_tr_b16 v[208:209], v246 offset:36864\n"
    "ds_read_b64_tr_b16 v[210:211], v246 offset:37888\n"
    "s_waitcnt lgkmcnt(12)\n"
    "v_mfma_f32_32x32x16_bf16 v[0:15], a[28:31], v[216:219], v[0:15]\n"
    "ds_read_b64_tr_b16 v[212:213], v246 offset:37376\n"
    "ds_read_b64_tr_b16 v[214:215], v246 offset:38400\n"
    "buffer_load_dwordx4 a[92:95], v239, s[52:55], s61 offen offset:3072 sc1\n"
    "s_waitcnt lgkmcnt(12)\n"
    "v_mfma_f32_32x32x16_bf16 v[16:31], a[28:31], v[220:223], v[16:31]\n"
    "ds_read_b64_tr_b16 v[216:217], v246 offset:38912\n"
    "ds_read_b64_tr_b16 v[218:219], v246 offset:39936\n"
    "s_bitcmp1_b32 s63, 3\n"
    "s_cbranch_scc1 .Lmha_FWA4S\n"
    ".Lmha_FWA4:\n"
    "s_xor_b32 s60, s91, 4\n"
    "s_lshl_b32 s60, s60, 13\n"
    "s_add_u32 s60, s60, s82\n"
    "s_add_u32 s61, s60, 0x1000\n"
    "s_waitcnt vmcnt(14)\n"
    "s_waitcnt lgkmcnt(12)\n"
    "v_mfma_f32_32x32x16_bf16 v[0:15], a[32:35], v[192:195], v[0:15]\n"
    "ds_read_b64_tr_b16 v[220:221], v246 offset:39424\n"
    "ds_read_b64_tr_b16 v[222:223], v246 offset:40448\n"
    "buffer_load_dwordx4 a[96:99], v239, s[52:55], s60 offen offset:0 sc1\n"
    "s_waitcnt lgkmcnt(12)\n"
    "v_mfma_f32_32x32x16_bf16 v[16:31], a[32:35], v[196:199], v[16:31]\n"
    "ds_read_b64_tr_b16 v[192:193], v246 offset:40960\n"
    "ds_read_b64_tr_b16 v[194:195], v246 offset:41984\n"
    "s_waitcnt lgkmcnt(12)\n"
    "v_mfma_f32_32x32x16_bf16 v[0:15], a[36:39], v[200:203], v[0:15]\n"
    "ds_read_b64_tr_b16 v[196:197], v246 offset:41472\n"
    "ds_read_b64_tr_b16 v[198:199], v246 offset:42496\n"
    "buffer_load_dwordx4 a[100:103], v239, s[52:55], s60 offen offset:1024 sc1\n"
    "s_waitcnt lgkmcnt(12)\n"
    "v_mfma_f32_32x32x16_bf16 v[16:31], a[36:39], v[204:207], v[16:31]\n"
    "ds_read_b64_tr_b16 v[200:201], v246 offset:43008\n"
    "ds_read_b64_tr_b16 v[202:203], v246 offset:44032\n"
    "s_waitcnt vmcnt(14)\n"
    "s_waitcnt lgkmcnt(12)\n"
    "v_mfma_f32_32x32x16_bf16 v[0:15], a[40:43], v[208:211], v[0:15]\n"
    "ds_read_b64_tr_b16 v[204:205], v246 offset:43520\n"
    "ds_read_b64_tr_b16 v[206:207], v246 offset:44544\n"
    "buffer_load_dwordx4 a[104:107], v239, s[52:55], s60 offen offset:2048 sc1\n"
    "s_waitcnt lgkmcnt(12)\n"
    "v_mfma_f32_32x32x16_bf16 v[16:31], a[40:43], v[212:215], v[16:31]\n"
    "ds_read_b64_tr_b16 v[208:209], v246 offset:45056\n"
    "ds_read_b64_tr_b16 v[210:211], v246 offset:46080\n"
    "s_waitcnt lgkmcnt(12)\n"
    "v_mfma_f32_32x32x16_bf16 v[0:15], a[44:47], v[216:219], v[0:15]\n"
    "ds_read_b64_tr_b16 v[212:213], v246 offset:45568\n"
    "ds_read_b64_tr_b16 v[214:215], v246 offset:46592\n"
    "ds_read_b32 v252, v225\n"
    "buffer_load_dwordx4 a[108:111], v239, s[52:55], s60 offen offset:3072 sc1\n"
    "s_waitcnt lgkmcnt(13)\n"
    "v_mfma_f32_32x32x16_bf16 v[16:31], a[44:47], v[220:223], v[16:31]\n"
    "ds_read_b64_tr_b16 v[216:217], v246 offset:47104\n"
    "ds_read_b64_tr_b16 v[218:219], v246 offset:48128\n"
    "s_waitcnt vmcnt(14)\n"
    "s_waitcnt lgkmcnt(13)\n"
    "v_mfma_f32_32x32x16_bf16 v[0:15], a[48:51], v[192:195], v[0:15]\n"
    "ds_read_b64_tr_b16 v[220:221], v246 offset:47616\n"
    "ds_read_b64_tr_b16 v[222:223], v246 offset:48640\n"
    "buffer_load_dwordx4 a[112:115], v239, s[52:55], s61 offen offset:0 sc1\n"
    "s_waitcnt lgkmcnt(13)\n"
    "v_mfma_f32_32x32x16_bf16 v[16:31], a[48:51], v[196:199], v[16:31]\n"
    "s_waitcnt lgkmcnt(4)\n"
    "v_readfirstlane_b32 s72, v252\n"
    "s_mov_b32 s74, 96\n"
    "s_cmp_ge_u32 s72, s74\n"
    "s_cbranch_scc0 .Lmha_GA2S\n"
    ".Lmha_GA2:\n"
    "ds_read_b64_tr_b16 v[192:193], v246 offset:49152\n"
    "ds_read_b64_tr_b16 v[194:195], v246 offset:50176\n"
    "v_mfma_f32_32x32x16_bf16 v[0:15], a[52:55], v[200:203], v[0:15]\n"
    "ds_read_b64_tr_b16 v[196:197], v246 offset:49664\n"
    "ds_read_b64_tr_b16 v[198:199], v246 offset:50688\n"
    "buffer_load_dwordx4 a[116:119], v239, s[52:55], s61 offen offset:1024 sc1\n"
    "v_mfma_f32_32x32x16_bf16 v[16:31], a[52:55], v[204:207], v[16:31]\n"
    "ds_read_b64_tr_b16 v[200:201], v246 offset:51200\n"
    "ds_read_b64_tr_b16 v[202:203], v246 offset:52224\n"
    "s_waitcnt vmcnt(14)\n"
    "v_mfma_f32_32x32x16_bf16 v[0:15], a[56:59], v[208:211], v[0:15]\n"
    "ds_read_b64_tr_b16 v[204:205], v246 offset:51712\n"
    "ds_read_b64_tr_b16 v[206:207], v246 offset:52736\n"
    "buffer_load_dwordx4 a[120:123], v239, s[52:55], s61 offen offset:2048 sc1\n"
    "v_mfma_f32_32x32x16_bf16 v[16:31], a[56:59], v[212:215], v[16:31]\n"
    "ds_read_b64_tr_b16 v[208:209], v246 offset:53248\n"
    "ds_read_b64_tr_b16 v[210:211], v246 offset:54272\n"
    "s_waitcnt lgkmcnt(12)\n"
    "v_mfma_f32_32x32x16_bf16 v[0:15], a[60:63], v[216:219], v[0:15]\n"
    "ds_read_b64_tr_b16 v[212:213], v246 offset:53760\n"
    "ds_read_b64_tr_b16 v[214:215], v246 offset:54784\n"
    "buffer_load_dwordx4 a[124:127], v239, s[52:55], s61 offen offset:3072 sc1\n"
    "s_waitcnt lgkmcnt(12)\n"
    "v_mfma_f32_32x32x16_bf16 v[16:31], a[60:63], v[220:223], v[16:31]\n"
    "ds_read_b64_tr_b16 v[216:217], v246 offset:55296\n"
    "ds_read_b64_tr_b16 v[218:219], v246 offset:56320\n"
    "s_bitcmp1_b32 s63, 4\n"
    "s_cbranch_scc1 .Lmha_FWA5S\n"
    ".Lmha_FWA5:\n"
    "s_xor_b32 s60, s91, 5\n"
    "s_lshl_b32 s60, s60, 13\n"
    "s_add_u32 s60, s60, s82\n"
    "s_add_u32 s61, s60, 0x1000\n"
    "s_waitcnt vmcnt(14)\n"
    "s_waitcnt lgkmcnt(12)\n"
    "v_mfma_f32_32x32x16_bf16 v[0:15], a[64:67], v[192:195], v[0:15]\n"
    "ds_read_b64_tr_b16 v[220:221], v246 offset:55808\n"
    "ds_read_b64_tr_b16 v[222:223], v246 offset:56832\n"
    "buffer_load_dwordx4 a[128:131], v239, s[52:55], s60 offen offset:0 sc1\n"
    "s_waitcnt lgkmcnt(12)\n"
    "v_mfma_f32_32x32x16_bf16 v[16:31], a[64:67], v[196:199], v[16:31]\n"
    "ds_read_b64_tr_b16 v[192:193], v246 offset:57344\n"
    "ds_read_b64_tr_b16 v[194:195], v246 offset:58368\n"
    "s_waitcnt lgkmcnt(12)\n"
    "v_mfma_f32_32x32x16_bf16 v[0:15], a[68:71], v[200:203], v[0:15]\n"
    "ds_read_b64_tr_b16 v[196:197], v246 offset:57856\n"
    "ds_read_b64_tr_b16 v[198:199], v246 offset:58880\n"
    "buffer_load_dwordx4 a[132:135], v239, s[52:55], s60 offen offset:1024 sc1\n"
    "s_waitcnt lgkmcnt(12)\n"
    "v_mfma_f32_32x32x16_bf16 v[16:31], a[68:71], v[204:207], v[16:31]\n"
    "ds_read_b64_tr_b16 v[200:201], v246 offset:59392\n"
    "ds_read_b64_tr_b16 v[202:203], v246 offset:60416\n"
    "s_waitcnt vmcnt(14)\n"
    "s_waitcnt lgkmcnt(12)\n"
    "v_mfma_f32_32x32x16_bf16 v[0:15], a[72:75], v[208:211], v[0:15]\n"
    "ds_read_b64_tr_b16 v[204:205], v246 offset:59904\n"
    "ds_read_b64_tr_b16 v[206:207], v246 offset:60928\n"
    "buffer_load_dwordx4 a[136:139], v239, s[52:55], s60 offen offset:2048 sc1\n"
    "s_waitcnt lgkmcnt(12)\n"
    "v_mfma_f32_32x32x16_bf16 v[16:31], a[72:75], v[212:215], v[16:31]\n"
    "ds_read_b64_tr_b16 v[208:209], v246 offset:61440\n"
    "ds_read_b64_tr_b16 v[210:211], v246 offset:62464\n"
    "s_waitcnt lgkmcnt(12)\n"
    "v_mfma_f32_32x32x16_bf16 v[0:15], a[76:79], v[216:219], v[0:15]\n"
    "ds_read_b64_tr_b16 v[212:213], v246 offset:61952\n"
    "ds_read_b64_tr_b16 v[214:215], v246 offset:62976\n"
    "buffer_load_dwordx4 a[140:143], v239, s[52:55], s60 offen offset:3072 sc1\n"
    "s_waitcnt lgkmcnt(12)\n"
    "v_mfma_f32_32x32x16_bf16 v[16:31], a[76:79], v[220:223], v[16:31]\n"
    "ds_read_b64_tr_b16 v[216:217], v246 offset:63488\n"
    "ds_read_b64_tr_b16 v[218:219], v246 offset:64512\n"
    "s_waitcnt vmcnt(14)\n"
    "s_waitcnt lgkmcnt(12)\n"
    "v_mfma_f32_32x32x16_bf16 v[0:15], a[80:83], v[192:195], v[0:15]\n"
    "ds_read_b64_tr_b16 v[220:221], v246 offset:64000\n"
    "ds_read_b64_tr_b16 v[222:223], v246 offset:65024\n"
    "buffer_load_dwordx4 a[144:147], v239, s[52:55], s61 offen offset:0 sc1\n"
    "s_waitcnt lgkmcnt(12)\n"
    "v_mfma_f32_32x32x16_bf16 v[16:31], a[80:83], v[196:199], v[16:31]\n"
    "ds_read_b64_tr_b16 v[192:193], v241 offset:16384\n"
    "ds_read_b64_tr_b16 v[194:195], v241 offset:17408\n"
    "s_waitcnt lgkmcnt(12)\n"
    "v_mfma_f32_32x32x16_bf16 v[0:15], a[84:87], v[200:203], v[0:15]\n"
    "ds_read_b64_tr_b16 v[196:197], v241 offset:16896\n"
    "ds_read_b64_tr_b16 v[198:199], v241 offset:17920\n"
    "buffer_load_dwordx4 a[148:151], v239, s[52:55], s61 offen offset:1024 sc1\n"
    "s_waitcnt lgkmcnt(12)\n"
    "v_mfma_f32_32x32x16_bf16 v[16:31], a[84:87], v[204:207], v[16:31]\n"
    "ds_read_b64_tr_b16 v[200:201], v241 offset:18432\n"
    "ds_read_b64_tr_b16 v[202:203], v241 offset:19456\n"
    "s_waitcnt vmcnt(14)\n"
    "s_waitcnt lgkmcnt(12)\n"
    "v_mfma_f32_32x32x16_bf16 v[0:15], a[88:91], v[208:211], v[0:15]\n"
    "ds_read_b64_tr_b16 v[204:205], v241 offset:18944\n"
    "ds_read_b64_tr_b16 v[206:207], v241 offset:19968\n"
    "buffer_load_dwordx4 a[152:155], v239, s[52:55], s61 offen offset:2048 sc1\n"
    "s_waitcnt lgkmcnt(12)\n"
    "v_mfma_f32_32x32x16_bf16 v[16:31], a[88:91], v[212:215], v[16:31]\n"
    "ds_read_b64_tr_b16 v[208:209], v241 offset:20480\n"
    "ds_read_b64_tr_b16 v[210:211], v241 offset:21504\n"
    "s_waitcnt lgkmcnt(12)\n"
    "v_mfma_f32_32x32x16_bf16 v[0:15], a[92:95], v[216:219], v[0:15]\n"
    "ds_read_b64_tr_b16 v[212:213], v241 offset:20992\n"
    "ds_read_b64_tr_b16 v[214:215], v241 offset:22016\n"
    "buffer_load_dwordx4 a[156:159], v239, s[52:55], s61 offen offset:3072 sc1\n"
    "s_waitcnt lgkmcnt(12)\n"
    "v_mfma_f32_32x32x16_bf16 v[16:31], a[92:95], v[220:223], v[16:31]\n"
    "ds_read_b64_tr_b16 v[216:217], v241 offset:22528\n"
    "ds_read_b64_tr_b16 v[218:219], v241 offset:23552\n"
    "s_bitcmp1_b32 s63, 5\n"
    "s_cbranch_scc1 .Lmha_FWA6S\n"
    ".Lmha_FWA6:\n"
    "s_xor_b32 s60, s91, 6\n"
    "s_lshl_b32 s60, s60, 13\n"
    "s_add_u32 s60, s60, s82\n"
    "s_add_u32 s61, s60, 0x1000\n"
    "s_waitcnt vmcnt(14)\n"
    "s_waitcnt lgkmcnt(12)\n"
    "v_mfma_f32_32x32x16_bf16 v[32:47], a[96:99], v[192:195], 0\n"
    "ds_read_b64_tr_b16 v[220:221], v241 offset:23040\n"
    "ds_read_b64_tr_b16 v[222:223], v241 offset:24064\n"
    "buffer_load_dwordx4 a[160:163], v239, s[52:55], s60 offen offset:0 sc1\n"
    "s_waitcnt lgkmcnt(12)\n"
    "v_mfma_f32_32x32x16_bf16 v[48:63], a[96:99], v[196:199], 0\n"
    "ds_read_b64_tr_b16 v[192:193], v241 offset:24576\n"
    "ds_read_b64_tr_b16 v[194:195], v241 offset:25600\n"
    "s_waitcnt lgkmcnt(12)\n"
    "v_mfma_f32_32x32x16_bf16 v[32:47], a[100:103], v[200:203], v[32:47]\n"
    "ds_read_b64_tr_b16 v[196:197], v241 offset:25088\n"
    "ds_read_b64_tr_b16 v[198:199], v241 offset:26112\n"
    "buffer_load_dwordx4 a[164:167], v239, s[52:55], s60 offen offset:1024 sc1\n"
    "s_waitcnt lgkmcnt(12)\n"
    "v_mfma_f32_32x32x16_bf16 v[48:63], a[100:103], v[204:207], v[48:63]\n"
    "ds_read_b64_tr_b16 v[200:201], v241 offset:26624\n"
    "ds_read_b64_tr_b16 v[202:203], v241 offset:27648\n"
    "s_waitcnt vmcnt(14)\n"
    "s_waitcnt lgkmcnt(12)\n"
    "v_mfma_f32_32x32x16_bf16 v[32:47], a[104:107], v[208:211], v[32:47]\n"
    "ds_read_b64_tr_b16 v[204:205], v241 offset:27136\n"
    "ds_read_b64_tr_b16 v[206:207], v241 offset:28160\n"
    "buffer_load_dwordx4 a[168:171], v239, s[52:55], s60 offen offset:2048 sc1\n"
    "s_waitcnt lgkmcnt(12)\n"
    "v_mfma_f32_32x32x16_bf16 v[48:63], a[104:107], v[212:215], v[48:63]\n"
    "ds_read_b64_tr_b16 v[208:209], v241 offset:28672\n"
    "ds_read_b64_tr_b16 v[210:211], v241 offset:29696\n"
    "s_waitcnt lgkmcnt(12)\n"
    "v_mfma_f32_32x32x16_bf16 v[32:47], a[108:111], v[216:219], v[32:47]\n"
    "ds_read_b64_tr_b16 v[212:213], v241 offset:29184\n"
    "ds_read_b64_tr_b16 v[214:215], v241 offset:30208\n"
    "buffer_load_dwordx4 a[172:175], v239, s[52:55], s60 offen offset:3072 sc1\n"
    "s_waitcnt lgkmcnt(12)\n"
    "v_mfma_f32_32x32x16_bf16 v[48:63], a[108:111], v[220:223], v[48:63]\n"
    "ds_read_b64_tr_b16 v[216:217], v241 offset:30720\n"
    "ds_read_b64_tr_b16 v[218:219], v241 offset:31744\n"
    "s_waitcnt vmcnt(14)\n"
    "s_waitcnt lgkmcnt(12)\n"
    "v_mfma_f32_32x32x16_bf16 v[32:47], a[112:115], v[192:195], v[32:47]\n"
    "ds_read_b64_tr_b16 v[220:221], v241 offset:31232\n"
    "ds_read_b64_tr_b16 v[222:223], v241 offset:32256\n"
    "buffer_load_dwordx4 a[176:179], v239, s[52:55], s61 offen offset:0 sc1\n"
    "s_waitcnt lgkmcnt(12)\n"
    "v_mfma_f32_32x32x16_bf16 v[48:63], a[112:115], v[196:199], v[48:63]\n"
    "ds_read_b64_tr_b16 v[192:193], v241 offset:32768\n"
    "ds_read_b64_tr_b16 v[194:195], v241 offset:33792\n"
    "s_waitcnt lgkmcnt(12)\n"
    "v_mfma_f32_32x32x16_bf16 v[32:47], a[116:119], v[200:203], v[32:47]\n"
    "ds_read_b64_tr_b16 v[196:197], v241 offset:33280\n"
    "ds_read_b64_tr_b16 v[198:199], v241 offset:34304\n"
    "buffer_load_dwordx4 a[180:183], v239, s[52:55], s61 offen offset:1024 sc1\n"
    "s_waitcnt lgkmcnt(12)\n"
    "v_mfma_f32_32x32x16_bf16 v[48:63], a[116:119], v[204:207], v[48:63]\n"
    "ds_read_b64_tr_b16 v[200:201], v241 offset:34816\n"
    "ds_read_b64_tr_b16 v[202:203], v241 offset:35840\n"
    "s_waitcnt vmcnt(14)\n"
    "s_waitcnt lgkmcnt(12)\n"
    "v_mfma_f32_32x32x16_bf16 v[32:47], a[120:123], v[208:211], v[32:47]\n"
    "ds_read_b64_tr_b16 v[204:205], v241 offset:35328\n"
    "ds_read_b64_tr_b16 v[206:207], v241 offset:36352\n"
    "buffer_load_dwordx4 a[184:187], v239, s[52:55], s61 offen offset:2048 sc1\n"
    "s_waitcnt lgkmcnt(12)\n"
    "v_mfma_f32_32x32x16_bf16 v[48:63], a[120:123], v[212:215], v[48:63]\n"
    "ds_read_b64_tr_b16 v[208:209], v241 offset:36864\n"
    "ds_read_b64_tr_b16 v[210:211], v241 offset:37888\n"
    "s_waitcnt lgkmcnt(12)\n"
    "v_mfma_f32_32x32x16_bf16 v[32:47], a[124:127], v[216:219], v[32:47]\n"
    "ds_read_b64_tr_b16 v[212:213], v241 offset:37376\n"
    "ds_read_b64_tr_b16 v[214:215], v241 offset:38400\n"
    "buffer_load_dwordx4 a[188:191], v239, s[52:55], s61 offen offset:3072 sc1\n"
    "s_waitcnt lgkmcnt(12)\n"
    "v_mfma_f32_32x32x16_bf16 v[48:63], a[124:127], v[220:223], v[48:63]\n"
    "ds_read_b64_tr_b16 v[216:217], v241 offset:38912\n"
    "ds_read_b64_tr_b16 v[218:219], v241 offset:39936\n"
    "s_bitcmp1_b32 s63, 6\n"
    "s_cbranch_scc1 .Lmha_FWA7S\n"
    ".Lmha_FWA7:\n"
    "s_xor_b32 s60, s91, 7\n"
    "s_lshl_b32 s60, s60, 13\n"
    "s_add_u32 s60, s60, s82\n"
    "s_add_u32 s61, s60, 0x1000\n"
    "s_waitcnt vmcnt(14)\n"
    "s_waitcnt lgkmcnt(12)\n"
    "v_mfma_f32_32x32x16_bf16 v[32:47], a[128:131], v[192:195], v[32:47]\n"
    "ds_read_b64_tr_b16 v[220:221], v241 offset:39424\n"
    "ds_read_b64_tr_b16 v[222:223], v241 offset:40448\n"
    "buffer_load_dwordx4 a[192:195], v239, s[52:55], s60 offen offset:0 sc1\n"
    "s_waitcnt lgkmcnt(12)\n"
    "v_mfma_f32_32x32x16_bf16 v[48:63], a[128:131], v[196:199], v[48:63]\n"
    "ds_read_b64_tr_b16 v[192:193], v241 offset:40960\n"
    "ds_read_b64_tr_b16 v[194:195], v241 offset:41984\n"
    "s_waitcnt lgkmcnt(12)\n"
    "v_mfma_f32_32x32x16_bf16 v[32:47], a[132:135], v[200:203], v[32:47]\n"
    "ds_read_b64_tr_b16 v[196:197], v241 offset:41472\n"
    "ds_read_b64_tr_b16 v[198:199], v241 offset:42496\n"
    "buffer_load_dwordx4 a[196:199], v239, s[52:55], s60 offen offset:1024 sc1\n"
    "s_waitcnt lgkmcnt(12)\n"
    "v_mfma_f32_32x32x16_bf16 v[48:63], a[132:135], v[204:207], v[48:63]\n"
    "ds_read_b64_tr_b16 v[200:201], v241 offset:43008\n"
    "ds_read_b64_tr_b16 v[202:203], v241 offset:44032\n"
    "s_waitcnt vmcnt(14)\n"
    "s_waitcnt lgkmcnt(12)\n"
    "v_mfma_f32_32x32x16_bf16 v[32:47], a[136:139], v[208:211], v[32:47]\n"
    "ds_read_b64_tr_b16 v[204:205], v241 offset:43520\n"
    "ds_read_b64_tr_b16 v[206:207], v241 offset:44544\n"
    "buffer_load_dwordx4 a[200:203], v239, s[52:55], s60 offen offset:2048 sc1\n"
    "s_waitcnt lgkmcnt(12)\n"
    "v_mfma_f32_32x32x16_bf16 v[48:63], a[136:139], v[212:215], v[48:63]\n"
    "ds_read_b64_tr_b16 v[208:209], v241 offset:45056\n"
    "ds_read_b64_tr_b16 v[210:211], v241 offset:46080\n"
    "s_waitcnt lgkmcnt(12)\n"
    "v_mfma_f32_32x32x16_bf16 v[32:47], a[140:143], v[216:219], v[32:47]\n"
    "ds_read_b64_tr_b16 v[212:213], v241 offset:45568\n"
    "ds_read_b64_tr_b16 v[214:215], v241 offset:46592\n"
    "buffer_load_dwordx4 a[204:207], v239, s[52:55], s60 offen offset:3072 sc1\n"
    "s_waitcnt lgkmcnt(12)\n"
    "v_mfma_f32_32x32x16_bf16 v[48:63], a[140:143], v[220:223], v[48:63]\n"
    "ds_read_b64_tr_b16 v[216:217], v241 offset:47104\n"
    "ds_read_b64_tr_b16 v[218:219], v241 offset:48128\n"
    "s_waitcnt vmcnt(14)\n"
    "s_waitcnt lgkmcnt(12)\n"
    "v_mfma_f32_32x32x16_bf16 v[32:47], a[144:147], v[192:195], v[32:47]\n"
    "ds_read_b64_tr_b16 v[220:221], v241 offset:47616\n"
    "ds_read_b64_tr_b16 v[222:223], v241 offset:48640\n"
    "buffer_load_dwordx4 a[208:211], v239, s[52:55], s61 offen offset:0 sc1\n"
    "s_waitcnt lgkmcnt(12)\n"
    "v_mfma_f32_32x32x16_bf16 v[48:63], a[144:147], v[196:199], v[48:63]\n"
    "ds_read_b64_tr_b16 v[192:193], v241 offset:49152\n"
    "ds_read_b64_tr_b16 v[194:195], v241 offset:50176\n"
    "s_waitcnt lgkmcnt(12)\n"
    "v_mfma_f32_32x32x16_bf16 v[32:47], a[148:151], v[200:203], v[32:47]\n"
    "ds_read_b64_tr_b16 v[196:197], v241 offset:49664\n"
    "ds_read_b64_tr_b16 v[198:199], v241 offset:50688\n"
    "buffer_load_dwordx4 a[212:215], v239, s[52:55], s61 offen offset:1024 sc1\n"
    "s_waitcnt lgkmcnt(12)\n"
    "v_mfma_f32_32x32x16_bf16 v[48:63], a[148:151], v[204:207], v[48:63]\n"
    "ds_read_b64_tr_b16 v[200:201], v241 offset:51200\n"
    "ds_read_b64_tr_b16 v[202:203], v241 offset:52224\n"
    "s_waitcnt vmcnt(14)\n"
    "s_waitcnt lgkmcnt(12)\n"
    "v_mfma_f32_32x32x16_bf16 v[32:47], a[152:155], v[208:211], v[32:47]\n"
    "ds_read_b64_tr_b16 v[204:205], v241 offset:51712\n"
    "ds_read_b64_tr_b16 v[206:207], v241 offset:52736\n"
    "buffer_load_dwordx4 a[216:219], v239, s[52:55], s61 offen offset:2048 sc1\n"
    "s_waitcnt lgkmcnt(12)\n"
    "v_mfma_f32_32x32x16_bf16 v[48:63], a[152:155], v[212:215], v[48:63]\n"
    "ds_read_b64_tr_b16 v[208:209], v241 offset:53248\n"
    "ds_read_b64_tr_b16 v[210:211], v241 offset:54272\n"
    "s_waitcnt lgkmcnt(12)\n"
    "v_mfma_f32_32x32x16_bf16 v[32:47], a[156:159], v[216:219], v[32:47]\n"
    "ds_read_b64_tr_b16 v[212:213], v241 offset:53760\n"
    "ds_read_b64_tr_b16 v[214:215], v241 offset:54784\n"
    "buffer_load_dwordx4 a[220:223], v239, s[52:55], s61 offen offset:3072 sc1\n"
    "s_waitcnt lgkmcnt(12)\n"
    "v_mfma_f32_32x32x16_bf16 v[48:63], a[156:159], v[220:223], v[48:63]\n"
    "ds_read_b64_tr_b16 v[216:217], v241 offset:55296\n"
    "ds_read_b64_tr_b16 v[218:219], v241 offset:56320\n"
    "s_waitcnt vmcnt(14)\n"
    "s_waitcnt lgkmcnt(12)\n"
    "v_mfma_f32_32x32x16_bf16 v[32:47], a[160:163], v[192:195], v[32:47]\n"
    "ds_read_b64_tr_b16 v[220:221], v241 offset:55808\n"
    "ds_read_b64_tr_b16 v[222:223], v241 offset:56832\n"
    "s_waitcnt lgkmcnt(12)\n"
    "v_mfma_f32_32x32x16_bf16 v[48:63], a[160:163], v[196:199], v[48:63]\n"
    "ds_read_b64_tr_b16 v[192:193], v241 offset:57344\n"
    "ds_read_b64_tr_b16 v[194:195], v241 offset:58368\n"
    "s_waitcnt lgkmcnt(12)\n"
    "v_mfma_f32_32x32x16_bf16 v[32:47], a[164:167], v[200:203], v[32:47]\n"
    "ds_read_b64_tr_b16 v[196:197], v241 offset:57856\n"
    "ds_read_b64_tr_b16 v[198:199], v241 offset:58880\n"
    "s_waitcnt lgkmcnt(12)\n"
    "v_mfma_f32_32x32x16_bf16 v[48:63], a[164:167], v[204:207], v[48:63]\n"
    "ds_read_b64_tr_b16 v[200:201], v241 offset:59392\n"
    "ds_read_b64_tr_b16 v[202:203], v241 offset:60416\n"
    "s_waitcnt vmcnt(12)\n"
    "s_waitcnt lgkmcnt(12)\n"
    "v_mfma_f32_32x32x16_bf16 v[32:47], a[168:171], v[208:211], v[32:47]\n"
    "ds_read_b64_tr_b16 v[204:205], v241 offset:59904\n"
    "ds_read_b64_tr_b16 v[206:207], v241 offset:60928\n"
    "s_waitcnt lgkmcnt(12)\n"
    "v_mfma_f32_32x32x16_bf16 v[48:63], a[168:171], v[212:215], v[48:63]\n"
    "ds_read_b64_tr_b16 v[208:209], v241 offset:61440\n"
    "ds_read_b64_tr_b16 v[210:211], v241 offset:62464\n"
    "s_waitcnt lgkmcnt(12)\n"
    "v_mfma_f32_32x32x16_bf16 v[32:47], a[172:175], v[216:219], v[32:47]\n"
    "ds_read_b64_tr_b16 v[212:213], v241 offset:61952\n"
    "ds_read_b64_tr_b16 v[214:215], v241 offset:62976\n"
    "s_waitcnt lgkmcnt(12)\n"
    "v_mfma_f32_32x32x16_bf16 v[48:63], a[172:175], v[220:223], v[48:63]\n"
    "ds_read_b64_tr_b16 v[216:217], v241 offset:63488\n"
    "ds_read_b64_tr_b16 v[218:219], v241 offset:64512\n"
    "s_waitcnt vmcnt(10)\n"
    "s_waitcnt lgkmcnt(12)\n"
    "v_mfma_f32_32x32x16_bf16 v[32:47], a[176:179], v[192:195], v[32:47]\n"
    "ds_read_b64_tr_b16 v[220:221], v241 offset:64000\n"
    "ds_read_b64_tr_b16 v[222:223], v241 offset:65024\n"
    "s_waitcnt lgkmcnt(12)\n"
    "v_mfma_f32_32x32x16_bf16 v[48:63], a[176:179], v[196:199], v[48:63]\n"
    "ds_read_b64_tr_b16 v[192:193], v246 offset:0\n"
    "ds_read_b64_tr_b16 v[194:195], v246 offset:1024\n"
    "s_waitcnt lgkmcnt(12)\n"
    "v_mfma_f32_32x32x16_bf16 v[32:47], a[180:183], v[200:203], v[32:47]\n"
    "ds_read_b64_tr_b16 v[196:197], v246 offset:512\n"
    "ds_read_b64_tr_b16 v[198:199], v246 offset:1536\n"
    "s_waitcnt lgkmcnt(12)\n"
    "v_mfma_f32_32x32x16_bf16 v[48:63], a[180:183], v[204:207], v[48:63]\n"
    "ds_read_b64_tr_b16 v[200:201], v246 offset:2048\n"
    "ds_read_b64_tr_b16 v[202:203], v246 offset:3072\n"
    "s_waitcnt vmcnt(8)\n"
    "s_waitcnt lgkmcnt(12)\n"
    "v_mfma_f32_32x32x16_bf16 v[32:47], a[184:187], v[208:211], v[32:47]\n"
    "ds_read_b64_tr_b16 v[204:205], v246 offset:2560\n"
    "ds_read_b64_tr_b16 v[206:207], v246 offset:3584\n"
    "s_waitcnt lgkmcnt(12)\n"
    "v_mfma_f32_32x32x16_bf16 v[48:63], a[184:187], v[212:215], v[48:63]\n"
    "ds_read_b64_tr_b16 v[208:209], v246 offset:4096\n"
    "ds_read_b64_tr_b16 v[210:211], v246 offset:5120\n"
    "s_waitcnt lgkmcnt(12)\n"
    "v_mfma_f32_32x32x16_bf16 v[32:47], a[188:191], v[216:219], v[32:47]\n"
    "ds_read_b64_tr_b16 v[212:213], v246 offset:4608\n"
    "ds_read_b64_tr_b16 v[214:215], v246 offset:5632\n"
    "s_waitcnt lgkmcnt(12)\n"
    "v_mfma_f32_32x32x16_bf16 v[48:63], a[188:191], v[220:223], v[48:63]\n"
    "ds_read_b64_tr_b16 v[216:217], v246 offset:6144\n"
    "ds_read_b64_tr_b16 v[218:219], v246 offset:7168\n"
    "s_waitcnt vmcnt(6)\n"
    "s_waitcnt lgkmcnt(12)\n"
    "v_mfma_f32_32x32x16_bf16 v[32:47], a[192:195], v[192:195], v[32:47]\n"
    "ds_read_b64_tr_b16 v[220:221], v246 offset:6656\n"
    "ds_read_b64_tr_b16 v[222:223], v246 offset:7680\n"
    "s_waitcnt lgkmcnt(12)\n"
    "v_mfma_f32_32x32x16_bf16 v[48:63], a[192:195], v[196:199], v[48:63]\n"
    "ds_read_b64_tr_b16 v[192:193], v246 offset:8192\n"
    "ds_read_b64_tr_b16 v[194:195], v246 offset:9216\n"
    "s_waitcnt lgkmcnt(12)\n"
    "v_mfma_f32_32x32x16_bf16 v[32:47], a[196:199], v[200:203], v[32:47]\n"
    "ds_read_b64_tr_b16 v[196:197], v246 offset:8704\n"
    "ds_read_b64_tr_b16 v[198:199], v246 offset:9728\n"
    "s_waitcnt lgkmcnt(12)\n"
    "v_mfma_f32_32x32x16_bf16 v[48:63], a[196:199], v[204:207], v[48:63]\n"
    "ds_read_b64_tr_b16 v[200:201], v246 offset:10240\n"
    "ds_read_b64_tr_b16 v[202:203], v246 offset:11264\n"
    "s_waitcnt vmcnt(4)\n"
    "s_waitcnt lgkmcnt(12)\n"
    "v_mfma_f32_32x32x16_bf16 v[32:47], a[200:203], v[208:211], v[32:47]\n"
    "ds_read_b64_tr_b16 v[204:205], v246 offset:10752\n"
    "ds_read_b64_tr_b16 v[206:207], v246 offset:11776\n"
    "s_waitcnt lgkmcnt(12)\n"
    "v_mfma_f32_32x32x16_bf16 v[48:63], a[200:203], v[212:215], v[48:63]\n"
    "ds_read_b64_tr_b16 v[208:209], v246 offset:12288\n"
    "ds_read_b64_tr_b16 v[210:211], v246 offset:13312\n"
    "s_waitcnt lgkmcnt(12)\n"
    "v_mfma_f32_32x32x16_bf16 v[32:47], a[204:207], v[216:219], v[32:47]\n"
    "ds_read_b64_tr_b16 v[212:213], v246 offset:12800\n"
    "ds_read_b64_tr_b16 v[214:215], v246 offset:13824\n"
    "s_waitcnt lgkmcnt(12)\n"
    "v_mfma_f32_32x32x16_bf16 v[48:63], a[204:207], v[220:223], v[48:63]\n"
    "ds_read_b64_tr_b16 v[216:217], v246 offset:14336\n"
    "ds_read_b64_tr_b16 v[218:219], v246 offset:15360\n"
    "s_waitcnt vmcnt(2)\n"
    "s_waitcnt lgkmcnt(12)\n"
    "v_mfma_f32_32x32x16_bf16 v[32:47], a[208:211], v[192:195], v[32:47]\n"
    "ds_read_b64_tr_b16 v[220:221], v246 offset:14848\n"
    "ds_read_b64_tr_b16 v[222:223], v246 offset:15872\n"
    "s_waitcnt lgkmcnt(12)\n"
    "v_mfma_f32_32x32x16_bf16 v[48:63], a[208:211], v[196:199], v[48:63]\n"
    "s_waitcnt lgkmcnt(10)\n"
    "v_mfma_f32_32x32x16_bf16 v[32:47], a[212:215], v[200:203], v[32:47]\n"
    "s_waitcnt lgkmcnt(8)\n"
    "v_mfma_f32_32x32x16_bf16 v[48:63], a[212:215], v[204:207], v[48:63]\n"
    "s_waitcnt vmcnt(0)\n"
    "s_waitcnt lgkmcnt(6)\n"
    "v_mfma_f32_32x32x16_bf16 v[32:47], a[216:219], v[208:211], v[32:47]\n"
    "s_waitcnt lgkmcnt(4)\n"
    "v_mfma_f32_32x32x16_bf16 v[48:63], a[216:219], v[212:215], v[48:63]\n"
    "s_waitcnt lgkmcnt(2)\n"
    "v_mfma_f32_32x32x16_bf16 v[32:47], a[220:223], v[216:219], v[32:47]\n"
    "s_waitcnt lgkmcnt(0)\n"
    "v_mfma_f32_32x32x16_bf16 v[48:63], a[220:223], v[220:223], v[48:63]\n"
    ".Lmha_GDONE:\n"
    "s_and_b32 s60, s63, 0x700\n"
    "s_cbranch_scc1 .Lmha_FW9S\n"
    ".Lmha_FW9:\n"
    "s_add_u32 s61, s67, 0x1000\n"
    "s_mov_b32 s62, -2\n"
    "s_nop 6\n"
    "v_pk_add_f32 v[4:5], v[4:5], v[36:37]\n"
    "v_pk_add_f32 v[6:7], v[6:7], v[38:39]\n"
    "v_pk_add_f32 v[20:21], v[20:21], v[52:53]\n"
    "v_pk_add_f32 v[22:23], v[22:23], v[54:55]\n"
    "v_cvt_pk_bf16_f32 v68, v4, v5\n"
    "v_cvt_pk_bf16_f32 v69, v6, v7\n"
    "v_cvt_pk_bf16_f32 v70, v20, v21\n"
    "v_cvt_pk_bf16_f32 v71, v22, v23\n"
    "v_min_u32 v68, s62, v68\n"
    "v_min_u32 v69, s62, v69\n"
    "v_min_u32 v70, s62, v70\n"
    "v_min_u32 v71, s62, v71\n"
    "buffer_store_dwordx4 v[68:71], v239, s[52:55], s64 offen sc1\n"
    "v_pk_add_f32 v[8:9], v[8:9], v[40:41]\n"
    "v_pk_add_f32 v[10:11], v[10:11], v[42:43]\n"
    "v_pk_add_f32 v[24:25], v[24:25], v[56:57]\n"
    "v_pk_add_f32 v[26:27], v[26:27], v[58:59]\n"
    "v_cvt_pk_bf16_f32 v72, v8, v9\n"
    "v_cvt_pk_bf16_f32 v73, v10, v11\n"
    "v_cvt_pk_bf16_f32 v74, v24, v25\n"
    "v_cvt_pk_bf16_f32 v75, v26, v27\n"
    "v_min_u32 v72, s62, v72\n"
    "v_min_u32 v73, s62, v73\n"
    "v_min_u32 v74, s62, v74\n"
    "v_min_u32 v75, s62, v75\n"
    "buffer_store_dwordx4 v[72:75], v239, s[52:55], s65 offen sc1\n"
    "v_pk_add_f32 v[12:13], v[12:13], v[44:45]\n"
    "v_pk_add_f32 v[14:15], v[14:15], v[46:47]\n"
    "v_pk_add_f32 v[28:29], v[28:29], v[60:61]\n"
    "v_pk_add_f32 v[30:31], v[30:31], v[62:63]\n"
    "v_cvt_pk_bf16_f32 v76, v12, v13\n"
    "v_cvt_pk_bf16_f32 v77, v14, v15\n"
    "v_cvt_pk_bf16_f32 v78, v28, v29\n"
    "v_cvt_pk_bf16_f32 v79, v30, v31\n"
    "v_min_u32 v76, s62, v76\n"
    "v_min_u32 v77, s62, v77\n"
    "v_min_u32 v78, s62, v78\n"
    "v_min_u32 v79, s62, v79\n"
    "buffer_store_dwordx4 v[76:79], v239, s[52:55], s66 offen sc1\n"
    "v_pk_add_f32 v[0:1], v[0:1], v[32:33]\n"
    "v_pk_add_f32 v[2:3], v[2:3], v[34:35]\n"
    "v_pk_add_f32 v[16:17], v[16:17], v[48:49]\n"
    "v_pk_add_f32 v[18:19], v[18:19], v[50:51]\n"
    ".Lmha_XPOLL:\n"
    "buffer_load_dwordx4 v[96:99], v239, s[52:55], s67 offen offset:2048 sc1\n"
    "buffer_load_dwordx4 v[100:103], v239, s[52:55], s61 offen offset:0 sc1\n"
    "buffer_load_dwordx4 v[104:107], v239, s[52:55], s61 offen offset:2048 sc1\n"
    "s_sub_u32 s79, s79, 1\n"
    "s_max_i32 s79, s79, 0\n"
    "s_waitcnt vmcnt(0)\n"
    "s_cbranch_scc0 .Lmha_XPOLLX\n"
    "v_max3_u32 v236, v96, v97, v98\n"
    "v_max3_u32 v237, v99, v100, v101\n"
    "v_max3_u32 v236, v236, v102, v103\n"
    "v_max3_u32 v237, v237, v104, v105\n"
    "v_max3_u32 v236, v236, v106, v107\n"
    "v_max_u32 v236, v236, v237\n"
    "v_cmp_eq_u32 vcc, -1, v236\n"
    "s_cbranch_vccz .Lmha_XPOLLX\n"
    "s_branch .Lmha_XPOLL\n"
    ".Lmha_XPOLLX:\n"
    "v_lshlrev_b32 v40, 16, v96\n"
    "v_and_b32 v41, 0xffff0000, v96\n"
    "v_lshlrev_b32 v42, 16, v97\n"
    "v_and_b32 v43, 0xffff0000, v97\n"
    "v_lshlrev_b32 v44, 16, v98\n"
    "v_and_b32 v45, 0xffff0000, v98\n"
    "v_lshlrev_b32 v46, 16, v99\n"
    "v_and_b32 v47, 0xffff0000, v99\n"
    "v_pk_add_f32 v[0:1], v[0:1], v[40:41]\n"
    "v_pk_add_f32 v[2:3], v[2:3], v[42:43]\n"
    "v_pk_add_f32 v[16:17], v[16:17], v[44:45]\n"
    "v_pk_add_f32 v[18:19], v[18:19], v[46:47]\n"
    "v_lshlrev_b32 v40, 16, v100\n"
    "v_and_b32 v41, 0xffff0000, v100\n"
    "v_lshlrev_b32 v42, 16, v101\n"
    "v_and_b32 v43, 0xffff0000, v101\n"
    "v_lshlrev_b32 v44, 16, v102\n"
    "v_and_b32 v45, 0xffff0000, v102\n"
    "v_lshlrev_b32 v46, 16, v103\n"
    "v_and_b32 v47, 0xffff0000, v103\n"
    "v_pk_add_f32 v[0:1], v[0:1], v[40:41]\n"
    "v_pk_add_f32 v[2:3], v[2:3], v[42:43]\n"
    "v_pk_add_f32 v[16:17], v[16:17], v[44:45]\n"
    "v_pk_add_f32 v[18:19], v[18:19], v[46:47]\n"
    "v_lshlrev_b32 v40, 16, v104\n"
    "v_and_b32 v41, 0xffff0000, v104\n"
    "v_lshlrev_b32 v42, 16, v105\n"
    "v_and_b32 v43, 0xffff0000, v105\n"
    "v_lshlrev_b32 v44, 16, v106\n"
    "v_and_b32 v45, 0xffff0000, v106\n"
    "v_lshlrev_b32 v46, 16, v107\n"
    "v_and_b32 v47, 0xffff0000, v107\n"
    "v_pk_add_f32 v[0:1], v[0:1], v[40:41]\n"
    "v_pk_add_f32 v[2:3], v[2:3], v[42:43]\n"
    "v_pk_add_f32 v[16:17], v[16:17], v[44:45]\n"
    "v_pk_add_f32 v[18:19], v[18:19], v[46:47]\n"
    "s_mov_b32 vcc_lo, 0xaaaaaaaa\n"
    "s_mov_b32 vcc_hi, 0xaaaaaaaa\n"
    "v_cndmask_b32 v40, v16, v0, vcc\n"
    "v_cndmask_b32 v41, v17, v1, vcc\n"
    "v_cndmask_b32 v42, v18, v2, vcc\n"
    "v_cndmask_b32 v43, v19, v3, vcc\n"
    "s_nop 1\n"
    "v_mov_b32_dpp v44, v40 quad_perm:[1,0,3,2] row_mask:0xf bank_mask:0xf\n"
    "v_mov_b32_dpp v45, v41 quad_perm:[1,0,3,2] row_mask:0xf bank_mask:0xf\n"
    "v_mov_b32_dpp v46, v42 quad_perm:[1,0,3,2] row_mask:0xf bank_mask:0xf\n"
    "v_mov_b32_dpp v47, v43 quad_perm:[1,0,3,2] row_mask:0xf bank_mask:0xf\n"
    "v_cndmask_b32 v48, v0, v44, vcc\n"
    "v_cndmask_b32 v52, v44, v16, vcc\n"
    "v_cndmask_b32 v49, v1, v45, vcc\n"
    "v_cndmask_b32 v53, v45, v17, vcc\n"
    "v_cndmask_b32 v50, v2, v46, vcc\n"
    "v_cndmask_b32 v54, v46, v18, vcc\n"
    "v_cndmask_b32 v51, v3, v47, vcc\n"
    "v_cndmask_b32 v55, v47, v19, vcc\n"
    "v_cvt_pk_bf16_f32 v32, v48, v52\n"
    "buffer_store_dword v32, v247, s[56:59], s71 offen offset:0 nt\n"
    "v_cvt_pk_bf16_f32 v33, v49, v53\n"
    "buffer_store_dword v33, v247, s[56:59], s71 offen offset:1024 nt\n"
    "v_cvt_pk_bf16_f32 v34, v50, v54\n"
    "buffer_store_dword v34, v247, s[56:59], s71 offen offset:2048 nt\n"
    "v_cvt_pk_bf16_f32 v35, v51, v55\n"
    "buffer_store_dword v35, v247, s[56:59], s71 offen offset:3072 nt\n"
    ".Lmha_FINISH:\n"
    "s_endpgm\n"
    ".Lmha_RARE0:\n"
    "v_mov_b32 v237, v236\n"
    "s_nop 1\n"
    "v_permlane32_swap_b32 v236, v237\n"
    "v_max3_f32 v236, v236, v237, 0\n"
    "v_exp_f32_e64 v238, -v236\n"
    "v_pk_add_f32 v[80:81], v[80:81], v[236:237] op_sel_hi:[1,0] neg_lo:[0,1] neg_hi:[0,1]\n"
    "v_pk_add_f32 v[82:83], v[82:83], v[236:237] op_sel_hi:[1,0] neg_lo:[0,1] neg_hi:[0,1]\n"
    "v_pk_add_f32 v[84:85], v[84:85], v[236:237] op_sel_hi:[1,0] neg_lo:[0,1] neg_hi:[0,1]\n"
    "v_pk_add_f32 v[86:87], v[86:87], v[236:237] op_sel_hi:[1,0] neg_lo:[0,1] neg_hi:[0,1]\n"
    "v_pk_add_f32 v[88:89], v[88:89], v[236:237] op_sel_hi:[1,0] neg_lo:[0,1] neg_hi:[0,1]\n"
    "v_pk_add_f32 v[90:91], v[90:91], v[236:237] op_sel_hi:[1,0] neg_lo:[0,1] neg_hi:[0,1]\n"
    "v_pk_add_f32 v[92:93], v[92:93], v[236:237] op_sel_hi:[1,0] neg_lo:[0,1] neg_hi:[0,1]\n"
    "v_pk_add_f32 v[94:95], v[94:95], v[236:237] op_sel_hi:[1,0] neg_lo:[0,1] neg_hi:[0,1]\n"
    "v_pk_add_f32 v[96:97], v[96:97], v[236:237] op_sel_hi:[1,0] neg_lo:[0,1] neg_hi:[0,1]\n"
    "v_pk_add_f32 v[98:99], v[98:99], v[236:237] op_sel_hi:[1,0] neg_lo:[0,1] neg_hi:[0,1]\n"
    "v_pk_add_f32 v[100:101], v[100:101], v[236:237] op_sel_hi:[1,0] neg_lo:[0,1] neg_hi:[0,1]\n"
    "v_pk_add_f32 v[102:103], v[102:103], v[236:237] op_sel_hi:[1,0] neg_lo:[0,1] neg_hi:[0,1]\n"
    "v_pk_add_f32 v[104:105], v[104:105], v[236:237] op_sel_hi:[1,0] neg_lo:[0,1] neg_hi:[0,1]\n"
    "v_pk_add_f32 v[106:107], v[106:107], v[236:237] op_sel_hi:[1,0] neg_lo:[0,1] neg_hi:[0,1]\n"
    "v_pk_add_f32 v[108:109], v[108:109], v[236:237] op_sel_hi:[1,0] neg_lo:[0,1] neg_hi:[0,1]\n"
    "v_pk_add_f32 v[110:111], v[110:111], v[236:237] op_sel_hi:[1,0] neg_lo:[0,1] neg_hi:[0,1]\n"
    "v_mul_f32 v233, v233, v238\n"
    "v_mul_f32 v234, v234, v238\n"
    "v_pk_mul_f32 v[0:1], v[0:1], v[238:239] op_sel_hi:[1,0]\n"
    "v_pk_mul_f32 v[2:3], v[2:3], v[238:239] op_sel_hi:[1,0]\n"
    "v_pk_mul_f32 v[4:5], v[4:5], v[238:239] op_sel_hi:[1,0]\n"
    "v_pk_mul_f32 v[6:7], v[6:7], v[238:239] op_sel_hi:[1,0]\n"
    "v_pk_mul_f32 v[8:9], v[8:9], v[238:239] op_sel_hi:[1,0]\n"
    "v_pk_mul_f32 v[10:11], v[10:11], v[238:239] op_sel_hi:[1,0]\n"
    "v_pk_mul_f32 v[12:13], v[12:13], v[238:239] op_sel_hi:[1,0]\n"
    "v_pk_mul_f32 v[14:15], v[14:15], v[238:239] op_sel_hi:[1,0]\n"
    "v_pk_mul_f32 v[16:17], v[16:17], v[238:239] op_sel_hi:[1,0]\n"
    "v_pk_mul_f32 v[18:19], v[18:19], v[238:239] op_sel_hi:[1,0]\n"
    "v_pk_mul_f32 v[20:21], v[20:21], v[238:239] op_sel_hi:[1,0]\n"
    "v_pk_mul_f32 v[22:23], v[22:23], v[238:239] op_sel_hi:[1,0]\n"
    "v_pk_mul_f32 v[24:25], v[24:25], v[238:239] op_sel_hi:[1,0]\n"
    "v_pk_mul_f32 v[26:27], v[26:27], v[238:239] op_sel_hi:[1,0]\n"
    "v_pk_mul_f32 v[28:29], v[28:29], v[238:239] op_sel_hi:[1,0]\n"
    "v_pk_mul_f32 v[30:31], v[30:31], v[238:239] op_sel_hi:[1,0]\n"
    "v_pk_mul_f32 v[32:33], v[32:33], v[238:239] op_sel_hi:[1,0]\n"
    "v_pk_mul_f32 v[34:35], v[34:35], v[238:239] op_sel_hi:[1,0]\n"
    "v_pk_mul_f32 v[36:37], v[36:37], v[238:239] op_sel_hi:[1,0]\n"
    "v_pk_mul_f32 v[38:39], v[38:39], v[238:239] op_sel_hi:[1,0]\n"
    "v_pk_mul_f32 v[40:41], v[40:41], v[238:239] op_sel_hi:[1,0]\n"
    "v_pk_mul_f32 v[42:43], v[42:43], v[238:239] op_sel_hi:[1,0]\n"
    "v_pk_mul_f32 v[44:45], v[44:45], v[238:239] op_sel_hi:[1,0]\n"
    "v_pk_mul_f32 v[46:47], v[46:47], v[238:239] op_sel_hi:[1,0]\n"
    "v_pk_mul_f32 v[48:49], v[48:49], v[238:239] op_sel_hi:[1,0]\n"
    "v_pk_mul_f32 v[50:51], v[50:51], v[238:239] op_sel_hi:[1,0]\n"
    "v_pk_mul_f32 v[52:53], v[52:53], v[238:239] op_sel_hi:[1,0]\n"
    "v_pk_mul_f32 v[54:55], v[54:55], v[238:239] op_sel_hi:[1,0]\n"
    "v_pk_mul_f32 v[56:57], v[56:57], v[238:239] op_sel_hi:[1,0]\n"
    "v_pk_mul_f32 v[58:59], v[58:59], v[238:239] op_sel_hi:[1,0]\n"
    "v_pk_mul_f32 v[60:61], v[60:61], v[238:239] op_sel_hi:[1,0]\n"
    "v_pk_mul_f32 v[62:63], v[62:63], v[238:239] op_sel_hi:[1,0]\n"
    "s_nop 1\n"
    "s_branch .Lmha_BACK0\n"
    ".Lmha_RARE1:\n"
    "v_mov_b32 v237, v236\n"
    "s_nop 1\n"
    "v_permlane32_swap_b32 v236, v237\n"
    "v_max3_f32 v236, v236, v237, 0\n"
    "v_exp_f32_e64 v238, -v236\n"
    "v_pk_add_f32 v[64:65], v[64:65], v[236:237] op_sel_hi:[1,0] neg_lo:[0,1] neg_hi:[0,1]\n"
    "v_pk_add_f32 v[66:67], v[66:67], v[236:237] op_sel_hi:[1,0] neg_lo:[0,1] neg_hi:[0,1]\n"
    "v_pk_add_f32 v[68:69], v[68:69], v[236:237] op_sel_hi:[1,0] neg_lo:[0,1] neg_hi:[0,1]\n"
    "v_pk_add_f32 v[70:71], v[70:71], v[236:237] op_sel_hi:[1,0] neg_lo:[0,1] neg_hi:[0,1]\n"
    "v_pk_add_f32 v[72:73], v[72:73], v[236:237] op_sel_hi:[1,0] neg_lo:[0,1] neg_hi:[0,1]\n"
    "v_pk_add_f32 v[74:75], v[74:75], v[236:237] op_sel_hi:[1,0] neg_lo:[0,1] neg_hi:[0,1]\n"
    "v_pk_add_f32 v[76:77], v[76:77], v[236:237] op_sel_hi:[1,0] neg_lo:[0,1] neg_hi:[0,1]\n"
    "v_pk_add_f32 v[78:79], v[78:79], v[236:237] op_sel_hi:[1,0] neg_lo:[0,1] neg_hi:[0,1]\n"
    "v_pk_add_f32 v[96:97], v[96:97], v[236:237] op_sel_hi:[1,0] neg_lo:[0,1] neg_hi:[0,1]\n"
    "v_pk_add_f32 v[98:99], v[98:99], v[236:237] op_sel_hi:[1,0] neg_lo:[0,1] neg_hi:[0,1]\n"
    "v_pk_add_f32 v[100:101], v[100:101], v[236:237] op_sel_hi:[1,0] neg_lo:[0,1] neg_hi:[0,1]\n"
    "v_pk_add_f32 v[102:103], v[102:103], v[236:237] op_sel_hi:[1,0] neg_lo:[0,1] neg_hi:[0,1]\n"
    "v_pk_add_f32 v[104:105], v[104:105], v[236:237] op_sel_hi:[1,0] neg_lo:[0,1] neg_hi:[0,1]\n"
    "v_pk_add_f32 v[106:107], v[106:107], v[236:237] op_sel_hi:[1,0] neg_lo:[0,1] neg_hi:[0,1]\n"
    "v_pk_add_f32 v[108:109], v[108:109], v[236:237] op_sel_hi:[1,0] neg_lo:[0,1] neg_hi:[0,1]\n"
    "v_pk_add_f32 v[110:111], v[110:111], v[236:237] op_sel_hi:[1,0] neg_lo:[0,1] neg_hi:[0,1]\n"
    "v_mul_f32 v233, v233, v238\n"
    "v_mul_f32 v234, v234, v238\n"
    "v_pk_mul_f32 v[0:1], v[0:1], v[238:239] op_sel_hi:[1,0]\n"
    "v_pk_mul_f32 v[2:3], v[2:3], v[238:239] op_sel_hi:[1,0]\n"
    "v_pk_mul_f32 v[4:5], v[4:5], v[238:239] op_sel_hi:[1,0]\n"
    "v_pk_mul_f32 v[6:7], v[6:7], v[238:239] op_sel_hi:[1,0]\n"
    "v_pk_mul_f32 v[8:9], v[8:9], v[238:239] op_sel_hi:[1,0]\n"
    "v_pk_mul_f32 v[10:11], v[10:11], v[238:239] op_sel_hi:[1,0]\n"
    "v_pk_mul_f32 v[12:13], v[12:13], v[238:239] op_sel_hi:[1,0]\n"
    "v_pk_mul_f32 v[14:15], v[14:15], v[238:239] op_sel_hi:[1,0]\n"
    "v_pk_mul_f32 v[16:17], v[16:17], v[238:239] op_sel_hi:[1,0]\n"
    "v_pk_mul_f32 v[18:19], v[18:19], v[238:239] op_sel_hi:[1,0]\n"
    "v_pk_mul_f32 v[20:21], v[20:21], v[238:239] op_sel_hi:[1,0]\n"
    "v_pk_mul_f32 v[22:23], v[22:23], v[238:239] op_sel_hi:[1,0]\n"
    "v_pk_mul_f32 v[24:25], v[24:25], v[238:239] op_sel_hi:[1,0]\n"
    "v_pk_mul_f32 v[26:27], v[26:27], v[238:239] op_sel_hi:[1,0]\n"
    "v_pk_mul_f32 v[28:29], v[28:29], v[238:239] op_sel_hi:[1,0]\n"
    "v_pk_mul_f32 v[30:31], v[30:31], v[238:239] op_sel_hi:[1,0]\n"
    "v_pk_mul_f32 v[32:33], v[32:33], v[238:239] op_sel_hi:[1,0]\n"
    "v_pk_mul_f32 v[34:35], v[34:35], v[238:239] op_sel_hi:[1,0]\n"
    "v_pk_mul_f32 v[36:37], v[36:37], v[238:239] op_sel_hi:[1,0]\n"
    "v_pk_mul_f32 v[38:39], v[38:39], v[238:239] op_sel_hi:[1,0]\n"
    "v_pk_mul_f32 v[40:41], v[40:41], v[238:239] op_sel_hi:[1,0]\n"
    "v_pk_mul_f32 v[42:43], v[42:43], v[238:239] op_sel_hi:[1,0]\n"
    "v_pk_mul_f32 v[44:45], v[44:45], v[238:239] op_sel_hi:[1,0]\n"
    "v_pk_mul_f32 v[46:47], v[46:47], v[238:239] op_sel_hi:[1,0]\n"
    "v_pk_mul_f32 v[48:49], v[48:49], v[238:239] op_sel_hi:[1,0]\n"
    "v_pk_mul_f32 v[50:51], v[50:51], v[238:239] op_sel_hi:[1,0]\n"
    "v_pk_mul_f32 v[52:53], v[52:53], v[238:239] op_sel_hi:[1,0]\n"
    "v_pk_mul_f32 v[54:55], v[54:55], v[238:239] op_sel_hi:[1,0]\n"
    "v_pk_mul_f32 v[56:57], v[56:57], v[238:239] op_sel_hi:[1,0]\n"
    "v_pk_mul_f32 v[58:59], v[58:59], v[238:239] op_sel_hi:[1,0]\n"
    "v_pk_mul_f32 v[60:61], v[60:61], v[238:239] op_sel_hi:[1,0]\n"
    "v_pk_mul_f32 v[62:63], v[62:63], v[238:239] op_sel_hi:[1,0]\n"
    "s_nop 1\n"
    "s_branch .Lmha_BACK1\n"
    ".Lmha_CLR:\n"
    "s_add_u32 s61, s67, 0x1000\n"
    "v_mov_b64 v[120:121], -1\n"
    "v_mov_b64 v[122:123], -1\n"
    "buffer_store_dwordx4 v[120:123], v239, s[52:55], s67 offen offset:2048 sc1\n"
    "buffer_store_dwordx4 v[120:123], v239, s[52:55], s61 offen offset:0 sc1\n"
    "buffer_store_dwordx4 v[120:123], v239, s[52:55], s61 offen offset:2048 sc1\n"
    "s_branch .Lmha_CLRX\n"
    ".Lmha_QW0S:\n"
    "s_swappc_b64 s[84:85], s[96:97]\n"
    ".Lmha_QW0R:\n"
    "s_branch .Lmha_QW0\n"
    ".Lmha_QW1S:\n"
    "s_swappc_b64 s[84:85], s[96:97]\n"
    ".Lmha_QW1R:\n"
    "s_branch .Lmha_QW1\n"
    ".Lmha_QW2S:\n"
    "s_swappc_b64 s[84:85], s[96:97]\n"
    ".Lmha_QW2R:\n"
    "s_branch .Lmha_QW2\n"
    ".Lmha_QW3S:\n"
    "s_swappc_b64 s[84:85], s[96:97]\n"
    ".Lmha_QW3R:\n"
    "s_branch .Lmha_QW3\n"
    ".Lmha_QW4S:\n"
    "s_swappc_b64 s[84:85], s[96:97]\n"
    ".Lmha_QW4R:\n"
    "s_branch .Lmha_QW4\n"
    ".Lmha_QW5S:\n"
    "s_swappc_b64 s[84:85], s[96:97]\n"
    ".Lmha_QW5R:\n"
    "s_branch .Lmha_QW5\n"
    ".Lmha_QW6S:\n"
    "s_swappc_b64 s[84:85], s[96:97]\n"
    ".Lmha_QW6R:\n"
    "s_branch .Lmha_QW6\n"
    ".Lmha_QW7S:\n"
    "s_swappc_b64 s[84:85], s[96:97]\n"
    ".Lmha_QW7R:\n"
    "s_branch .Lmha_QW7\n"
    ".Lmha_AQ0S:\n"
    "s_swappc_b64 s[84:85], s[96:97]\n"
    ".Lmha_AQ0R:\n"
    "s_branch .Lmha_AQ0\n"
    ".Lmha_AQV0S:\n"
    "s_swappc_b64 s[84:85], s[96:97]\n"
    ".Lmha_AQV0R:\n"
    "s_branch .Lmha_AQV0\n"
    ".Lmha_AQK0S:\n"
    "s_swappc_b64 s[84:85], s[96:97]\n"
    ".Lmha_AQK0R:\n"
    "s_branch .Lmha_AQK0\n"
    ".Lmha_AQV1S:\n"
    "s_swappc_b64 s[84:85], s[96:97]\n"
    ".Lmha_AQV1R:\n"
    "s_branch .Lmha_AQV1\n"
    ".Lmha_AQK1S:\n"
    "s_swappc_b64 s[84:85], s[96:97]\n"
    ".Lmha_AQK1R:\n"
    "s_branch .Lmha_AQK1\n"
    ".Lmha_GB4S:\n"
    "s_swappc_b64 s[84:85], s[96:97]\n"
    ".Lmha_GB4R:\n"
    "s_branch .Lmha_GB4\n"
    ".Lmha_GB5S:\n"
    "s_swappc_b64 s[84:85], s[96:97]\n"
    ".Lmha_GB5R:\n"
    "s_branch .Lmha_GB5\n"
    ".Lmha_GB6S:\n"
    "s_swappc_b64 s[84:85], s[96:97]\n"
    ".Lmha_GB6R:\n"
    "s_branch .Lmha_GB6\n"
    ".Lmha_GA0S:\n"
    "s_swappc_b64 s[84:85], s[96:97]\n"
    ".Lmha_GA0R:\n"
    "s_branch .Lmha_GA0\n"
    ".Lmha_GA1S:\n"
    "s_swappc_b64 s[84:85], s[96:97]\n"
    ".Lmha_GA1R:\n"
    "s_branch .Lmha_GA1\n"
    ".Lmha_GA2S:\n"
    "s_swappc_b64 s[84:85], s[96:97]\n"
    ".Lmha_GA2R:\n"
    "s_branch .Lmha_GA2\n"
    ".Lmha_FWB4S:\n"
    "buffer_load_dwordx2 v[254:255], v244, s[52:55], s88 offen sc1\n"
    "s_sub_u32 s79, s79, 1\n"
    "s_max_i32 s79, s79, 0\n"
    "s_waitcnt vmcnt(0)\n"
    "s_cbranch_scc0 .Lmha_FWB4\n"
    "v_cmp_ne_u64 vcc, s[80:81], v[254:255]\n"
    "s_mov_b32 s63, vcc_lo\n"
    "s_bitcmp1_b32 s63, 3\n"
    "s_cbranch_scc0 .Lmha_FWB4\n"
    "s_add_u32 s18, s18, 1\n"
    "s_cmp_le_u32 s18, 3\n"
    "s_cbranch_scc1 .Lmha_FWB4S\n"
    "s_sleep 16\n"
    "s_branch .Lmha_FWB4S\n"
    ".Lmha_FWB5S:\n"
    "buffer_load_dwordx2 v[254:255], v244, s[52:55], s88 offen sc1\n"
    "s_sub_u32 s79, s79, 1\n"
    "s_max_i32 s79, s79, 0\n"
    "s_waitcnt vmcnt(0)\n"
    "s_cbranch_scc0 .Lmha_FWB5\n"
    "v_cmp_ne_u64 vcc, s[80:81], v[254:255]\n"
    "s_mov_b32 s63, vcc_lo\n"
    "s_bitcmp1_b32 s63, 4\n"
    "s_cbranch_scc0 .Lmha_FWB5\n"
    "s_add_u32 s18, s18, 1\n"
    "s_cmp_le_u32 s18, 3\n"
    "s_cbranch_scc1 .Lmha_FWB5S\n"
    "s_sleep 16\n"
    "s_branch .Lmha_FWB5S\n"
    ".Lmha_FWB6S:\n"
    "buffer_load_dwordx2 v[254:255], v244, s[52:55], s88 offen sc1\n"
    "s_sub_u32 s79, s79, 1\n"
    "s_max_i32 s79, s79, 0\n"
    "s_waitcnt vmcnt(0)\n"
    "s_cbranch_scc0 .Lmha_FWB6\n"
    "v_cmp_ne_u64 vcc, s[80:81], v[254:255]\n"
    "s_mov_b32 s63, vcc_lo\n"
    "s_bitcmp1_b32 s63, 5\n"
    "s_cbranch_scc0 .Lmha_FWB6\n"
    "s_add_u32 s18, s18, 1\n"
    "s_cmp_le_u32 s18, 3\n"
    "s_cbranch_scc1 .Lmha_FWB6S\n"
    "s_sleep 16\n"
    "s_branch .Lmha_FWB6S\n"
    ".Lmha_FWB7S:\n"
    "buffer_load_dwordx2 v[254:255], v244, s[52:55], s88 offen sc1\n"
    "s_sub_u32 s79, s79, 1\n"
    "s_max_i32 s79, s79, 0\n"
    "s_waitcnt vmcnt(0)\n"
    "s_cbranch_scc0 .Lmha_FWB7\n"
    "v_cmp_ne_u64 vcc, s[80:81], v[254:255]\n"
    "s_mov_b32 s63, vcc_lo\n"
    "s_bitcmp1_b32 s63, 6\n"
    "s_cbranch_scc0 .Lmha_FWB7\n"
    "s_add_u32 s18, s18, 1\n"
    "s_cmp_le_u32 s18, 3\n"
    "s_cbranch_scc1 .Lmha_FWB7S\n"
    "s_sleep 16\n"
    "s_branch .Lmha_FWB7S\n"
    ".Lmha_FWB1S:\n"
    "buffer_load_dwordx2 v[254:255], v244, s[52:55], s88 offen sc1\n"
    "s_sub_u32 s79, s79, 1\n"
    "s_max_i32 s79, s79, 0\n"
    "s_waitcnt vmcnt(0)\n"
    "s_cbranch_scc0 .Lmha_FWB1\n"
    "v_cmp_ne_u64 vcc, s[80:81], v[254:255]\n"
    "s_mov_b32 s63, vcc_lo\n"
    "s_bitcmp1_b32 s63, 0\n"
    "s_cbranch_scc0 .Lmha_FWB1\n"
    "s_add_u32 s18, s18, 1\n"
    "s_cmp_le_u32 s18, 3\n"
    "s_cbranch_scc1 .Lmha_FWB1S\n"
    "s_sleep 16\n"
    "s_branch .Lmha_FWB1S\n"
    ".Lmha_FWB2S:\n"
    "buffer_load_dwordx2 v[254:255], v244, s[52:55], s88 offen sc1\n"
    "s_sub_u32 s79, s79, 1\n"
    "s_max_i32 s79, s79, 0\n"
    "s_waitcnt vmcnt(0)\n"
    "s_cbranch_scc0 .Lmha_FWB2\n"
    "v_cmp_ne_u64 vcc, s[80:81], v[254:255]\n"
    "s_mov_b32 s63, vcc_lo\n"
    "s_bitcmp1_b32 s63, 1\n"
    "s_cbranch_scc0 .Lmha_FWB2\n"
    "s_add_u32 s18, s18, 1\n"
    "s_cmp_le_u32 s18, 3\n"
    "s_cbranch_scc1 .Lmha_FWB2S\n"
    "s_sleep 16\n"
    "s_branch .Lmha_FWB2S\n"
    ".Lmha_FWB3S:\n"
    "buffer_load_dwordx2 v[254:255], v244, s[52:55], s88 offen sc1\n"
    "s_sub_u32 s79, s79, 1\n"
    "s_max_i32 s79, s79, 0\n"
    "s_waitcnt vmcnt(0)\n"
    "s_cbranch_scc0 .Lmha_FWB3\n"
    "v_cmp_ne_u64 vcc, s[80:81], v[254:255]\n"
    "s_mov_b32 s63, vcc_lo\n"
    "s_bitcmp1_b32 s63, 2\n"
    "s_cbranch_scc0 .Lmha_FWB3\n"
    "s_add_u32 s18, s18, 1\n"
    "s_cmp_le_u32 s18, 3\n"
    "s_cbranch_scc1 .Lmha_FWB3S\n"
    "s_sleep 16\n"
    "s_branch .Lmha_FWB3S\n"
    ".Lmha_FWA1S:\n"
    "buffer_load_dwordx2 v[254:255], v244, s[52:55], s88 offen sc1\n"
    "s_sub_u32 s79, s79, 1\n"
    "s_max_i32 s79, s79, 0\n"
    "s_waitcnt vmcnt(0)\n"
    "s_cbranch_scc0 .Lmha_FWA1\n"
    "v_cmp_ne_u64 vcc, s[80:81], v[254:255]\n"
    "s_mov_b32 s63, vcc_lo\n"
    "s_bitcmp1_b32 s63, 0\n"
    "s_cbranch_scc0 .Lmha_FWA1\n"
    "s_add_u32 s18, s18, 1\n"
    "s_cmp_le_u32 s18, 3\n"
    "s_cbranch_scc1 .Lmha_FWA1S\n"
    "s_sleep 16\n"
    "s_branch .Lmha_FWA1S\n"
    ".Lmha_FWA2S:\n"
    "buffer_load_dwordx2 v[254:255], v244, s[52:55], s88 offen sc1\n"
    "s_sub_u32 s79, s79, 1\n"
    "s_max_i32 s79, s79, 0\n"
    "s_waitcnt vmcnt(0)\n"
    "s_cbranch_scc0 .Lmha_FWA2\n"
    "v_cmp_ne_u64 vcc, s[80:81], v[254:255]\n"
    "s_mov_b32 s63, vcc_lo\n"
    "s_bitcmp1_b32 s63, 1\n"
    "s_cbranch_scc0 .Lmha_FWA2\n"
    "s_add_u32 s18, s18, 1\n"
    "s_cmp_le_u32 s18, 3\n"
    "s_cbranch_scc1 .Lmha_FWA2S\n"
    "s_sleep 16\n"
    "s_branch .Lmha_FWA2S\n"
    ".Lmha_FWA3S:\n"
    "buffer_load_dwordx2 v[254:255], v244, s[52:55], s88 offen sc1\n"
    "s_sub_u32 s79, s79, 1\n"
    "s_max_i32 s79, s79, 0\n"
    "s_waitcnt vmcnt(0)\n"
    "s_cbranch_scc0 .Lmha_FWA3\n"
    "v_cmp_ne_u64 vcc, s[80:81], v[254:255]\n"
    "s_mov_b32 s63, vcc_lo\n"
    "s_bitcmp1_b32 s63, 2\n"
    "s_cbranch_scc0 .Lmha_FWA3\n"
    "s_add_u32 s18, s18, 1\n"
    "s_cmp_le_u32 s18, 3\n"
    "s_cbranch_scc1 .Lmha_FWA3S\n"
    "s_sleep 16\n"
    "s_branch .Lmha_FWA3S\n"
    ".Lmha_FWA4S:\n"
    "buffer_load_dwordx2 v[254:255], v244, s[52:55], s88 offen sc1\n"
    "s_sub_u32 s79, s79, 1\n"
    "s_max_i32 s79, s79, 0\n"
    "s_waitcnt vmcnt(0)\n"
    "s_cbranch_scc0 .Lmha_FWA4\n"
    "v_cmp_ne_u64 vcc, s[80:81], v[254:255]\n"
    "s_mov_b32 s63, vcc_lo\n"
    "s_bitcmp1_b32 s63, 3\n"
    "s_cbranch_scc0 .Lmha_FWA4\n"
    "s_add_u32 s18, s18, 1\n"
    "s_cmp_le_u32 s18, 3\n"
    "s_cbranch_scc1 .Lmha_FWA4S\n"
    "s_sleep 16\n"
    "s_branch .Lmha_FWA4S\n"
    ".Lmha_FWA5S:\n"
    "buffer_load_dwordx2 v[254:255], v244, s[52:55], s88 offen sc1\n"
    "s_sub_u32 s79, s79, 1\n"
    "s_max_i32 s79, s79, 0\n"
    "s_waitcnt vmcnt(0)\n"
    "s_cbranch_scc0 .Lmha_FWA5\n"
    "v_cmp_ne_u64 vcc, s[80:81], v[254:255]\n"
    "s_mov_b32 s63, vcc_lo\n"
    "s_bitcmp1_b32 s63, 4\n"
    "s_cbranch_scc0 .Lmha_FWA5\n"
    "s_add_u32 s18, s18, 1\n"
    "s_cmp_le_u32 s18, 3\n"
    "s_cbranch_scc1 .Lmha_FWA5S\n"
    "s_sleep 16\n"
    "s_branch .Lmha_FWA5S\n"
    ".Lmha_FWA6S:\n"
    "buffer_load_dwordx2 v[254:255], v244, s[52:55], s88 offen sc1\n"
    "s_sub_u32 s79, s79, 1\n"
    "s_max_i32 s79, s79, 0\n"
    "s_waitcnt vmcnt(0)\n"
    "s_cbranch_scc0 .Lmha_FWA6\n"
    "v_cmp_ne_u64 vcc, s[80:81], v[254:255]\n"
    "s_mov_b32 s63, vcc_lo\n"
    "s_bitcmp1_b32 s63, 5\n"
    "s_cbranch_scc0 .Lmha_FWA6\n"
    "s_add_u32 s18, s18, 1\n"
    "s_cmp_le_u32 s18, 3\n"
    "s_cbranch_scc1 .Lmha_FWA6S\n"
    "s_sleep 16\n"
    "s_branch .Lmha_FWA6S\n"
    ".Lmha_FWA7S:\n"
    "buffer_load_dwordx2 v[254:255], v244, s[52:55], s88 offen sc1\n"
    "s_sub_u32 s79, s79, 1\n"
    "s_max_i32 s79, s79, 0\n"
    "s_waitcnt vmcnt(0)\n"
    "s_cbranch_scc0 .Lmha_FWA7\n"
    "v_cmp_ne_u64 vcc, s[80:81], v[254:255]\n"
    "s_mov_b32 s63, vcc_lo\n"
    "s_bitcmp1_b32 s63, 6\n"
    "s_cbranch_scc0 .Lmha_FWA7\n"
    "s_add_u32 s18, s18, 1\n"
    "s_cmp_le_u32 s18, 3\n"
    "s_cbranch_scc1 .Lmha_FWA7S\n"
    "s_sleep 16\n"
    "s_branch .Lmha_FWA7S\n"
    ".Lmha_FW9S:\n"
    "buffer_load_dwordx2 v[254:255], v244, s[52:55], s88 offen sc1\n"
    "s_sub_u32 s79, s79, 1\n"
    "s_max_i32 s79, s79, 0\n"
    "s_waitcnt vmcnt(0)\n"
    "s_cbranch_scc0 .Lmha_FW9\n"
    "v_cmp_ne_u64 vcc, s[80:81], v[254:255]\n"
    "s_mov_b32 s63, vcc_lo\n"
    "s_and_b32 s60, s63, 0x700\n"
    "s_cbranch_scc0 .Lmha_FW9\n"
    "s_add_u32 s18, s18, 1\n"
    "s_cmp_le_u32 s18, 3\n"
    "s_cbranch_scc1 .Lmha_FW9S\n"
    "s_sleep 16\n"
    "s_branch .Lmha_FW9S\n"
    ".Lmha_TSPIN:\n"
    "ds_read_b32 v252, v225\n"
    "s_waitcnt lgkmcnt(0)\n"
    "v_readfirstlane_b32 s72, v252\n"
    "s_sub_u32 s79, s79, 1\n"
    "s_max_i32 s79, s79, 0\n"
    "s_cbranch_scc0 .Lmha_TSPINX\n"
    "s_cmp_ge_u32 s72, s74\n"
    "s_cbranch_scc1 .Lmha_TSPINX\n"
    "s_sleep 1\n"
    "s_branch .Lmha_TSPIN\n"
    ".Lmha_TSPINX:\n"
    "s_setpc_b64 s[84:85]\n"
    ".Lmha_LOADER2:\n"
    "s_lshl_b32 s60, s39, 5\n"
    "s_add_u32 s60, s60, s38\n"
    "s_lshl_b32 s60, s60, 18\n"
    "s_add_u32 s40, s22, s60\n"
    "s_addc_u32 s41, s23, 0\n"
    "s_and_b32 s41, s41, 0xffff\n"
    "s_mov_b32 s42, 0x40000\n"
    "s_add_u32 s44, s24, s60\n"
    "s_addc_u32 s45, s25, 0\n"
    "s_and_b32 s45, s45, 0xffff\n"
    "s_mov_b32 s46, 0x40000\n"
    "s_mov_b32 s47, 0x20000\n"
    "v_lshl_or_b32 v227, v235, 8, v238\n"
    "v_and_b32 v228, 16, v224\n"
    "v_xor_b32 v228, v228, v227\n"
    "v_xor_b32 v229, 32, v228\n"
    "s_lshl_b32 s60, s89, 5\n"
    "v_add_u32 v231, 0, v238\n"
    "v_and_b32 v231, 0x7f, v231\n"
    "v_lshl_or_b32 v231, v235, 10, v231\n"
    "v_add_u32 v240, 3, v240\n"
    "v_add_u32 v238, 32, v225\n"
    "s_getpc_b64 s[66:67]\n"
    ".Lmha_PC3:\n"
    "s_add_u32 s66, s66, .Lmha_ISSK-.Lmha_PC3\n"
    "s_addc_u32 s67, s67, 0\n"
    "s_getpc_b64 s[68:69]\n"
    ".Lmha_PC4:\n"
    "s_add_u32 s68, s68, .Lmha_ISSV-.Lmha_PC4\n"
    "s_addc_u32 s69, s69, 0\n"
    "s_getpc_b64 s[96:97]\n"
    ".Lmha_PC5:\n"
    "s_add_u32 s96, s96, .Lmha_MSPIN-.Lmha_PC5\n"
    "s_addc_u32 s97, s97, 0\n"
    "s_mov_b32 s64, 0\n"
    "s_swappc_b64 s[86:87], s[66:67]\n"
    ".Lmha_IR0:\n"
    "v_add_u32 v240, 1, v240\n"
    "ds_write_b32 v226, v240 offset:0\n"
    "s_mov_b32 s64, 8192\n"
    "s_swappc_b64 s[86:87], s[66:67]\n"
    ".Lmha_IR1:\n"
    "v_add_u32 v240, 1, v240\n"
    "ds_write_b32 v226, v240 offset:0\n"
    "s_mov_b32 s52, s32\n"
    "s_and_b32 s53, s33, 0xffff\n"
    "s_mov_b32 s48, s28\n"
    "s_and_b32 s49, s29, 0xffff\n"
    "s_mov_b32 s50, 0x400000\n"
    "s_mov_b32 s51, 0x20000\n"
    "s_mov_b32 s82, 0\n"
    "s_getpc_b64 s[70:71]\n"
    ".Lmha_PC6:\n"
    "s_add_u32 s70, s70, .Lmha_ISSB-.Lmha_PC6\n"
    "s_addc_u32 s71, s71, 0\n"
    ".Lmha_LLOOP:\n"
    "s_lshl_b32 s64, s73, 12\n"
    "s_sub_u32 s64, s64, 73728\n"
    "s_swappc_b64 s[86:87], s[68:69]\n"
    ".Lmha_IR2:\n"
    "v_add_u32 v240, 1, v240\n"
    "ds_write_b32 v226, v240 offset:0\n"
    "s_lshl_b32 s64, s73, 12\n"
    "s_sub_u32 s64, s64, 61440\n"
    "s_swappc_b64 s[86:87], s[66:67]\n"
    ".Lmha_IR3:\n"
    "v_add_u32 v240, 1, v240\n"
    "ds_write_b32 v226, v240 offset:0\n"
    "s_cmp_lt_u32 s73, 68\n"
    "s_cbranch_scc1 .Lmha_LLOOP\n"
    "s_add_u32 s66, s66, .Lmha_ISSK2-.Lmha_ISSK\n"
    "s_addc_u32 s67, s67, 0\n"
    "s_add_u32 s68, s68, .Lmha_ISSV2-.Lmha_ISSV\n"
    "s_addc_u32 s69, s69, 0\n"
    "s_mov_b32 s73, 68\n"
    "s_mov_b32 s64, 204800\n"
    "s_swappc_b64 s[86:87], s[68:69]\n"
    ".Lmha_IR4:\n"
    "s_mov_b32 s82, 0\n"
    "s_mov_b32 m0, 131072\n"
    "s_swappc_b64 s[86:87], s[70:71]\n"
    ".Lmha_IR5:\n"
    "s_mov_b32 s73, 69\n"
    "s_mov_b32 s64, 221184\n"
    "s_swappc_b64 s[86:87], s[66:67]\n"
    ".Lmha_IR6:\n"
    "v_mov_b32 v240, 66\n"
    "ds_write_b32 v226, v240 offset:0\n"
    "s_mov_b32 s82, 1\n"
    "s_mov_b32 m0, 139264\n"
    "s_swappc_b64 s[86:87], s[70:71]\n"
    ".Lmha_IR7:\n"
    "v_mov_b32 v240, 67\n"
    "ds_write_b32 v226, v240 offset:0\n"
    "s_mov_b32 s73, 70\n"
    "s_mov_b32 s64, 212992\n"
    "s_swappc_b64 s[86:87], s[68:69]\n"
    ".Lmha_IR8:\n"
    "v_mov_b32 v240, 68\n"
    "ds_write_b32 v226, v240 offset:0\n"
    "s_mov_b32 s73, 71\n"
    "s_mov_b32 s64, 229376\n"
    "s_swappc_b64 s[86:87], s[66:67]\n"
    ".Lmha_IR9:\n"
    "v_mov_b32 v240, 69\n"
    "ds_write_b32 v226, v240 offset:0\n"
    "s_mov_b32 s74, 67\n"
    "s_cmp_ge_u32 s72, s74\n"
    "s_cbranch_scc1 .Lmha_LF0\n"
    "s_swappc_b64 s[84:85], s[96:97]\n"
    ".Lmha_LF0:\n"
    "s_mov_b32 s82, 8\n"
    "s_mov_b32 m0, 16384\n"
    "s_swappc_b64 s[86:87], s[70:71]\n"
    ".Lmha_IR10:\n"
    "s_mov_b32 s73, 72\n"
    "s_mov_b32 s64, 221184\n"
    "s_swappc_b64 s[86:87], s[68:69]\n"
    ".Lmha_IR11:\n"
    "v_mov_b32 v240, 70\n"
    "ds_write_b32 v226, v240 offset:0\n"
    "s_mov_b32 s74, 68\n"
    "s_cmp_ge_u32 s72, s74\n"
    "s_cbranch_scc1 .Lmha_LF1\n"
    "s_swappc_b64 s[84:85], s[96:97]\n"
    ".Lmha_LF1:\n"
    "s_mov_b32 s82, 9\n"
    "s_mov_b32 m0, 24576\n"
    "s_swappc_b64 s[86:87], s[70:71]\n"
    ".Lmha_IR12:\n"
    "s_mov_b32 s73, 73\n"
    "s_mov_b32 s64, 237568\n"
    "s_swappc_b64 s[86:87], s[66:67]\n"
    ".Lmha_IR13:\n"
    "v_mov_b32 v240, 71\n"
    "ds_write_b32 v226, v240 offset:0\n"
    "s_mov_b32 s74, 69\n"
    "s_cmp_ge_u32 s72, s74\n"
    "s_cbranch_scc1 .Lmha_LF2\n"
    "s_swappc_b64 s[84:85], s[96:97]\n"
    ".Lmha_LF2:\n"
    "s_mov_b32 s82, 10\n"
    "s_mov_b32 m0, 32768\n"
    "s_swappc_b64 s[86:87], s[70:71]\n"
    ".Lmha_IR14:\n"
    "v_mov_b32 v240, 72\n"
    "ds_write_b32 v226, v240 offset:0\n"
    "s_mov_b32 s73, 74\n"
    "s_mov_b32 s64, 229376\n"
    "s_swappc_b64 s[86:87], s[68:69]\n"
    ".Lmha_IR15:\n"
    "s_mov_b32 s74, 70\n"
    "s_cmp_ge_u32 s72, s74\n"
    "s_cbranch_scc1 .Lmha_LF3\n"
    "s_swappc_b64 s[84:85], s[96:97]\n"
    ".Lmha_LF3:\n"
    "s_mov_b32 s82, 11\n"
    "s_mov_b32 m0, 40960\n"
    "s_swappc_b64 s[86:87], s[70:71]\n"
    ".Lmha_IR16:\n"
    "v_mov_b32 v240, 73\n"
    "ds_write_b32 v226, v240 offset:0\n"
    "s_mov_b32 s73, 75\n"
    "s_mov_b32 s64, 245760\n"
    "s_swappc_b64 s[86:87], s[66:67]\n"
    ".Lmha_IR17:\n"
    "s_mov_b32 s74, 71\n"
    "s_cmp_ge_u32 s72, s74\n"
    "s_cbranch_scc1 .Lmha_LF4\n"
    "s_swappc_b64 s[84:85], s[96:97]\n"
    ".Lmha_LF4:\n"
    "s_mov_b32 s82, 12\n"
    "s_mov_b32 m0, 49152\n"
    "s_swappc_b64 s[86:87], s[70:71]\n"
    ".Lmha_IR18:\n"
    "v_mov_b32 v240, 74\n"
    "ds_write_b32 v226, v240 offset:0\n"
    "s_mov_b32 s73, 76\n"
    "s_mov_b32 s64, 237568\n"
    "s_swappc_b64 s[86:87], s[68:69]\n"
    ".Lmha_IR19:\n"
    "s_mov_b32 s74, 72\n"
    "s_cmp_ge_u32 s72, s74\n"
    "s_cbranch_scc1 .Lmha_LF5\n"
    "s_swappc_b64 s[84:85], s[96:97]\n"
    ".Lmha_LF5:\n"
    "s_mov_b32 s82, 13\n"
    "s_mov_b32 m0, 57344\n"
    "s_swappc_b64 s[86:87], s[70:71]\n"
    ".Lmha_IR20:\n"
    "v_mov_b32 v240, 75\n"
    "ds_write_b32 v226, v240 offset:0\n"
    "s_mov_b32 s73, 77\n"
    "s_mov_b32 s64, 253952\n"
    "s_swappc_b64 s[86:87], s[66:67]\n"
    ".Lmha_IR21:\n"
    "s_mov_b32 s74, 73\n"
    "s_cmp_ge_u32 s72, s74\n"
    "s_cbranch_scc1 .Lmha_LF6\n"
    "s_swappc_b64 s[84:85], s[96:97]\n"
    ".Lmha_LF6:\n"
    "s_mov_b32 s82, 14\n"
    "s_mov_b32 m0, 65536\n"
    "s_swappc_b64 s[86:87], s[70:71]\n"
    ".Lmha_IR22:\n"
    "v_mov_b32 v240, 76\n"
    "ds_write_b32 v226, v240 offset:0\n"
    "s_mov_b32 s73, 78\n"
    "s_mov_b32 s64, 245760\n"
    "s_swappc_b64 s[86:87], s[68:69]\n"
    ".Lmha_IR23:\n"
    "s_mov_b32 s74, 74\n"
    "s_cmp_ge_u32 s72, s74\n"
    "s_cbranch_scc1 .Lmha_LF7\n"
    "s_swappc_b64 s[84:85], s[96:97]\n"
    ".Lmha_LF7:\n"
    "s_mov_b32 s82, 15\n"
    "s_mov_b32 m0, 73728\n"
    "s_swappc_b64 s[86:87], s[70:71]\n"
    ".Lmha_IR24:\n"
    "v_mov_b32 v240, 77\n"
    "ds_write_b32 v226, v240 offset:0\n"
    "s_mov_b32 s73, 80\n"
    "s_mov_b32 s64, 253952\n"
    "s_swappc_b64 s[86:87], s[68:69]\n"
    ".Lmha_IR25:\n"
    "s_mov_b32 s74, 75\n"
    "s_cmp_ge_u32 s72, s74\n"
    "s_cbranch_scc1 .Lmha_LF8\n"
    "s_swappc_b64 s[84:85], s[96:97]\n"
    ".Lmha_LF8:\n"
    "s_mov_b32 s82, 2\n"
    "s_mov_b32 m0, 81920\n"
    "s_swappc_b64 s[86:87], s[70:71]\n"
    ".Lmha_IR26:\n"
    "v_mov_b32 v240, 78\n"
    "ds_write_b32 v226, v240 offset:0\n"
    "s_mov_b32 s74, 76\n"
    "s_cmp_ge_u32 s72, s74\n"
    "s_cbranch_scc1 .Lmha_LF9\n"
    "s_swappc_b64 s[84:85], s[96:97]\n"
    ".Lmha_LF9:\n"
    "s_mov_b32 s82, 3\n"
    "s_mov_b32 m0, 90112\n"
    "s_swappc_b64 s[86:87], s[70:71]\n"
    ".Lmha_IR27:\n"
    "s_waitcnt vmcnt(32)\n"
    "v_mov_b32 v240, 80\n"
    "ds_write_b32 v226, v240 offset:0\n"
    "s_waitcnt vmcnt(24)\n"
    "s_waitcnt vmcnt(16)\n"
    "v_mov_b32 v240, 90\n"
    "ds_write_b32 v226, v240 offset:0\n"
    "s_mov_b32 s74, 999\n"
    "s_cmp_ge_u32 s72, s74\n"
    "s_cbranch_scc1 .Lmha_LF10\n"
    "s_swappc_b64 s[84:85], s[96:97]\n"
    ".Lmha_LF10:\n"
    "s_mov_b32 s82, 4\n"
    "s_mov_b32 m0, 98304\n"
    "s_swappc_b64 s[86:87], s[70:71]\n"
    ".Lmha_IR28:\n"
    "s_mov_b32 s74, 78\n"
    "s_cmp_ge_u32 s72, s74\n"
    "s_cbranch_scc1 .Lmha_LF11\n"
    "s_swappc_b64 s[84:85], s[96:97]\n"
    ".Lmha_LF11:\n"
    "s_mov_b32 s82, 5\n"
    "s_mov_b32 m0, 106496\n"
    "s_swappc_b64 s[86:87], s[70:71]\n"
    ".Lmha_IR29:\n"
    "s_mov_b32 s74, 79\n"
    "s_cmp_ge_u32 s72, s74\n"
    "s_cbranch_scc1 .Lmha_LF12\n"
    "s_swappc_b64 s[84:85], s[96:97]\n"
    ".Lmha_LF12:\n"
    "s_mov_b32 s82, 6\n"
    "s_mov_b32 m0, 114688\n"
    "s_swappc_b64 s[86:87], s[70:71]\n"
    ".Lmha_IR30:\n"
    "s_mov_b32 s74, 80\n"
    "s_cmp_ge_u32 s72, s74\n"
    "s_cbranch_scc1 .Lmha_LF13\n"
    "s_swappc_b64 s[84:85], s[96:97]\n"
    ".Lmha_LF13:\n"
    "s_mov_b32 s82, 7\n"
    "s_mov_b32 m0, 122880\n"
    "s_swappc_b64 s[86:87], s[70:71]\n"
    ".Lmha_IR31:\n"
    "s_waitcnt vmcnt(32)\n"
    "v_mov_b32 v240, 92\n"
    "ds_write_b32 v226, v240 offset:0\n"
    "s_waitcnt vmcnt(16)\n"
    "v_mov_b32 v240, 94\n"
    "ds_write_b32 v226, v240 offset:0\n"
    "s_waitcnt vmcnt(0)\n"
    "v_mov_b32 v240, 96\n"
    "ds_write_b32 v226, v240 offset:0\n"
    "s_cmp_lg_u32 s36, 0\n"
    "s_cbranch_scc1 .Lmha_FINISH\n"
    "buffer_load_dwordx2 v[242:243], off, s[52:55], 0 sc1\n"
    "v_lshlrev_b32 v236, 6, v224\n"
    "s_waitcnt vmcnt(0)\n"
    "v_readfirstlane_b32 s80, v242\n"
    "v_readfirstlane_b32 s81, v243\n"
    "s_add_u32 s80, s80, 0x7f4a7c15\n"
    "s_addc_u32 s81, s81, 0x9e3779b9\n"
    "v_mov_b64 v[242:243], s[80:81]\n"
    ".Lmha_BPOLL:\n"
    "s_mov_b32 s62, 4096\n"
    "buffer_load_dwordx2 v[0:1], v236, s[52:55], s62 offen sc1\n"
    "s_add_u32 s62, s62, 4096\n"
    "buffer_load_dwordx2 v[2:3], v236, s[52:55], s62 offen sc1\n"
    "s_add_u32 s62, s62, 4096\n"
    "buffer_load_dwordx2 v[4:5], v236, s[52:55], s62 offen sc1\n"
    "s_add_u32 s62, s62, 4096\n"
    "buffer_load_dwordx2 v[6:7], v236, s[52:55], s62 offen sc1\n"
    "s_add_u32 s62, s62, 4096\n"
    "buffer_load_dwordx2 v[8:9], v236, s[52:55], s62 offen sc1\n"
    "s_add_u32 s62, s62, 4096\n"
    "buffer_load_dwordx2 v[10:11], v236, s[52:55], s62 offen sc1\n"
    "s_add_u32 s62, s62, 4096\n"
    "buffer_load_dwordx2 v[12:13], v236, s[52:55], s62 offen sc1\n"
    "s_add_u32 s62, s62, 4096\n"
    "buffer_load_dwordx2 v[14:15], v236, s[52:55], s62 offen sc1\n"
    "s_add_u32 s62, s62, 4096\n"
    "buffer_load_dwordx2 v[16:17], v236, s[52:55], s62 offen sc1\n"
    "s_add_u32 s62, s62, 4096\n"
    "buffer_load_dwordx2 v[18:19], v236, s[52:55], s62 offen sc1\n"
    "s_add_u32 s62, s62, 4096\n"
    "buffer_load_dwordx2 v[20:21], v236, s[52:55], s62 offen sc1\n"
    "s_add_u32 s62, s62, 4096\n"
    "buffer_load_dwordx2 v[22:23], v236, s[52:55], s62 offen sc1\n"
    "s_add_u32 s62, s62, 4096\n"
    "s_waitcnt vmcnt(0)\n"
    "s_mov_b64 s[60:61], 0\n"
    "v_cmp_ne_u64 vcc, s[80:81], v[0:1]\n"
    "s_or_b64 s[60:61], s[60:61], vcc\n"
    "v_cmp_ne_u64 vcc, s[80:81], v[2:3]\n"
    "s_or_b64 s[60:61], s[60:61], vcc\n"
    "v_cmp_ne_u64 vcc, s[80:81], v[4:5]\n"
    "s_or_b64 s[60:61], s[60:61], vcc\n"
    "v_cmp_ne_u64 vcc, s[80:81], v[6:7]\n"
    "s_or_b64 s[60:61], s[60:61], vcc\n"
    "v_cmp_ne_u64 vcc, s[80:81], v[8:9]\n"
    "s_or_b64 s[60:61], s[60:61], vcc\n"
    "v_cmp_ne_u64 vcc, s[80:81], v[10:11]\n"
    "s_or_b64 s[60:61], s[60:61], vcc\n"
    "v_cmp_ne_u64 vcc, s[80:81], v[12:13]\n"
    "s_or_b64 s[60:61], s[60:61], vcc\n"
    "v_cmp_ne_u64 vcc, s[80:81], v[14:15]\n"
    "s_or_b64 s[60:61], s[60:61], vcc\n"
    "v_cmp_ne_u64 vcc, s[80:81], v[16:17]\n"
    "s_or_b64 s[60:61], s[60:61], vcc\n"
    "v_cmp_ne_u64 vcc, s[80:81], v[18:19]\n"
    "s_or_b64 s[60:61], s[60:61], vcc\n"
    "v_cmp_ne_u64 vcc, s[80:81], v[20:21]\n"
    "s_or_b64 s[60:61], s[60:61], vcc\n"
    "v_cmp_ne_u64 vcc, s[80:81], v[22:23]\n"
    "s_or_b64 s[60:61], s[60:61], vcc\n"
    "s_cbranch_scc0 .Lmha_BOK\n"
    "s_cmp_eq_u32 s79, 0\n"
    "s_cbranch_scc1 .Lmha_FINISH\n"
    "s_sub_u32 s79, s79, 1\n"
    "s_sleep 8\n"
    "s_branch .Lmha_BPOLL\n"
    ".Lmha_BOK:\n"
    "s_mov_b64 exec, 1\n"
    "buffer_store_dwordx2 v[242:243], off, s[52:55], 0 sc1\n"
    "s_mov_b64 exec, -1\n"
    "s_branch .Lmha_FINISH\n"
    ".Lmha_ISSK:\n"
    "s_sub_u32 s74, s73, 15\n"
    "s_cmp_ge_u32 s72, s74\n"
    "s_cbranch_scc1 .Lmha_ISSKF\n"
    "s_swappc_b64 s[84:85], s[96:97]\n"
    ".Lmha_ISSKF:\n"
    "s_lshl_b32 s62, s73, 13\n"
    "s_and_b32 m0, s62, s77\n"
    "s_waitcnt vmcnt(21)\n"
    "buffer_load_dwordx4 v228, s[40:43], s64 offen offset:0 nt lds\n"
    "s_waitcnt vmcnt(21)\n"
    "buffer_load_dwordx4 v229, s[40:43], s64 offen offset:2048 nt lds\n"
    "s_add_u32 m0, m0, 896\n"
    "s_waitcnt vmcnt(21)\n"
    "buffer_load_dwordx4 v228, s[40:43], s64 offen offset:128 nt lds\n"
    "s_waitcnt vmcnt(21)\n"
    "buffer_load_dwordx4 v229, s[40:43], s64 offen offset:2176 nt lds\n"
    "s_add_u32 m0, m0, 3200\n"
    "s_add_u32 s64, s64, 0x1000\n"
    "s_waitcnt vmcnt(21)\n"
    "buffer_load_dwordx4 v228, s[40:43], s64 offen offset:0 nt lds\n"
    "s_waitcnt vmcnt(21)\n"
    "buffer_load_dwordx4 v229, s[40:43], s64 offen offset:2048 nt lds\n"
    "s_add_u32 m0, m0, 896\n"
    "s_waitcnt vmcnt(21)\n"
    "buffer_load_dwordx4 v228, s[40:43], s64 offen offset:128 nt lds\n"
    "s_waitcnt vmcnt(21)\n"
    "buffer_load_dwordx4 v229, s[40:43], s64 offen offset:2176 nt lds\n"
    "s_add_u32 s73, s73, 1\n"
    "s_setpc_b64 s[86:87]\n"
    ".Lmha_ISSV:\n"
    "s_sub_u32 s74, s73, 15\n"
    "s_cmp_ge_u32 s72, s74\n"
    "s_cbranch_scc1 .Lmha_ISSVF\n"
    "s_swappc_b64 s[84:85], s[96:97]\n"
    ".Lmha_ISSVF:\n"
    "s_lshl_b32 s62, s73, 13\n"
    "s_and_b32 m0, s62, s77\n"
    "s_waitcnt vmcnt(21)\n"
    "buffer_load_dwordx4 v227, s[44:47], s64 offen offset:0 nt lds\n"
    "s_waitcnt vmcnt(21)\n"
    "buffer_load_dwordx4 v227, s[44:47], s64 offen offset:2048 nt lds\n"
    "s_add_u32 m0, m0, 896\n"
    "s_waitcnt vmcnt(21)\n"
    "buffer_load_dwordx4 v227, s[44:47], s64 offen offset:128 nt lds\n"
    "s_waitcnt vmcnt(21)\n"
    "buffer_load_dwordx4 v227, s[44:47], s64 offen offset:2176 nt lds\n"
    "s_add_u32 m0, m0, 3200\n"
    "s_add_u32 s64, s64, 0x1000\n"
    "s_waitcnt vmcnt(21)\n"
    "buffer_load_dwordx4 v227, s[44:47], s64 offen offset:0 nt lds\n"
    "s_waitcnt vmcnt(21)\n"
    "buffer_load_dwordx4 v227, s[44:47], s64 offen offset:2048 nt lds\n"
    "s_add_u32 m0, m0, 896\n"
    "s_waitcnt vmcnt(21)\n"
    "buffer_load_dwordx4 v227, s[44:47], s64 offen offset:128 nt lds\n"
    "s_waitcnt vmcnt(21)\n"
    "buffer_load_dwordx4 v227, s[44:47], s64 offen offset:2176 nt lds\n"
    "s_add_u32 s73, s73, 1\n"
    "s_setpc_b64 s[86:87]\n"
    ".Lmha_ISSK2:\n"
    "s_sub_u32 s74, s73, 15\n"
    "s_cmp_ge_u32 s72, s74\n"
    "s_cbranch_scc1 .Lmha_ISSK2F\n"
    "s_swappc_b64 s[84:85], s[96:97]\n"
    ".Lmha_ISSK2F:\n"
    "s_lshl_b32 s62, s73, 13\n"
    "s_and_b32 m0, s62, s77\n"
    "s_waitcnt vmcnt(39)\n"
    "buffer_load_dwordx4 v228, s[40:43], s64 offen offset:0 nt lds\n"
    "s_waitcnt vmcnt(39)\n"
    "buffer_load_dwordx4 v229, s[40:43], s64 offen offset:2048 nt lds\n"
    "s_add_u32 m0, m0, 896\n"
    "s_waitcnt vmcnt(39)\n"
    "buffer_load_dwordx4 v228, s[40:43], s64 offen offset:128 nt lds\n"
    "s_waitcnt vmcnt(39)\n"
    "buffer_load_dwordx4 v229, s[40:43], s64 offen offset:2176 nt lds\n"
    "s_add_u32 m0, m0, 3200\n"
    "s_add_u32 s64, s64, 0x1000\n"
    "s_waitcnt vmcnt(39)\n"
    "buffer_load_dwordx4 v228, s[40:43], s64 offen offset:0 nt lds\n"
    "s_waitcnt vmcnt(39)\n"
    "buffer_load_dwordx4 v229, s[40:43], s64 offen offset:2048 nt lds\n"
    "s_add_u32 m0, m0, 896\n"
    "s_waitcnt vmcnt(39)\n"
    "buffer_load_dwordx4 v228, s[40:43], s64 offen offset:128 nt lds\n"
    "s_waitcnt vmcnt(39)\n"
    "buffer_load_dwordx4 v229, s[40:43], s64 offen offset:2176 nt lds\n"
    "s_add_u32 s73, s73, 1\n"
    "s_setpc_b64 s[86:87]\n"
    ".Lmha_ISSV2:\n"
    "s_sub_u32 s74, s73, 15\n"
    "s_cmp_ge_u32 s72, s74\n"
    "s_cbranch_scc1 .Lmha_ISSV2F\n"
    "s_swappc_b64 s[84:85], s[96:97]\n"
    ".Lmha_ISSV2F:\n"
    "s_lshl_b32 s62, s73, 13\n"
    "s_and_b32 m0, s62, s77\n"
    "s_waitcnt vmcnt(39)\n"
    "buffer_load_dwordx4 v227, s[44:47], s64 offen offset:0 nt lds\n"
    "s_waitcnt vmcnt(39)\n"
    "buffer_load_dwordx4 v227, s[44:47], s64 offen offset:2048 nt lds\n"
    "s_add_u32 m0, m0, 896\n"
    "s_waitcnt vmcnt(39)\n"
    "buffer_load_dwordx4 v227, s[44:47], s64 offen offset:128 nt lds\n"
    "s_waitcnt vmcnt(39)\n"
    "buffer_load_dwordx4 v227, s[44:47], s64 offen offset:2176 nt lds\n"
    "s_add_u32 m0, m0, 3200\n"
    "s_add_u32 s64, s64, 0x1000\n"
    "s_waitcnt vmcnt(39)\n"
    "buffer_load_dwordx4 v227, s[44:47], s64 offen offset:0 nt lds\n"
    "s_waitcnt vmcnt(39)\n"
    "buffer_load_dwordx4 v227, s[44:47], s64 offen offset:2048 nt lds\n"
    "s_add_u32 m0, m0, 896\n"
    "s_waitcnt vmcnt(39)\n"
    "buffer_load_dwordx4 v227, s[44:47], s64 offen offset:128 nt lds\n"
    "s_waitcnt vmcnt(39)\n"
    "buffer_load_dwordx4 v227, s[44:47], s64 offen offset:2176 nt lds\n"
    "s_add_u32 s73, s73, 1\n"
    "s_setpc_b64 s[86:87]\n"
    ".Lmha_ISSB:\n"
    "s_lshl_b32 s61, s91, 1\n"
    "s_xor_b32 s60, s82, s61\n"
    "s_bfe_u32 s61, s60, 0x20001\n"
    "s_lshl_b32 s61, s61, 4\n"
    "s_bfe_u32 s62, s60, 0x10003\n"
    "s_lshl_b32 s62, s62, 1\n"
    "s_and_b32 s60, s60, 1\n"
    "s_or_b32 s60, s60, s61\n"
    "s_or_b32 s60, s60, s62\n"
    "s_lshl_b32 s61, s89, 2\n"
    "s_or_b32 s60, s60, s61\n"
    "s_lshl_b32 s60, s60, 16\n"
    "s_lshl_b32 s61, s91, 7\n"
    "s_or_b32 s64, s60, s61\n"
    "s_waitcnt vmcnt(39)\n"
    "buffer_load_dwordx4 v231, s[48:51], s64 offen lds\n"
    "s_add_u32 m0, m0, 0x400\n"
    "s_add_u32 s64, s64, 0x2000\n"
    "s_waitcnt vmcnt(39)\n"
    "buffer_load_dwordx4 v231, s[48:51], s64 offen lds\n"
    "s_add_u32 m0, m0, 0x400\n"
    "s_add_u32 s64, s64, 0x2000\n"
    "s_waitcnt vmcnt(39)\n"
    "buffer_load_dwordx4 v231, s[48:51], s64 offen lds\n"
    "s_add_u32 m0, m0, 0x400\n"
    "s_add_u32 s64, s64, 0x2000\n"
    "s_waitcnt vmcnt(39)\n"
    "buffer_load_dwordx4 v231, s[48:51], s64 offen lds\n"
    "s_add_u32 m0, m0, 0x400\n"
    "s_add_u32 s64, s64, 0x2000\n"
    "s_waitcnt vmcnt(39)\n"
    "buffer_load_dwordx4 v231, s[48:51], s64 offen lds\n"
    "s_add_u32 m0, m0, 0x400\n"
    "s_add_u32 s64, s64, 0x2000\n"
    "s_waitcnt vmcnt(39)\n"
    "buffer_load_dwordx4 v231, s[48:51], s64 offen lds\n"
    "s_add_u32 m0, m0, 0x400\n"
    "s_add_u32 s64, s64, 0x2000\n"
    "s_waitcnt vmcnt(39)\n"
    "buffer_load_dwordx4 v231, s[48:51], s64 offen lds\n"
    "s_add_u32 m0, m0, 0x400\n"
    "s_add_u32 s64, s64, 0x2000\n"
    "s_waitcnt vmcnt(39)\n"
    "buffer_load_dwordx4 v231, s[48:51], s64 offen lds\n"
    "s_setpc_b64 s[86:87]\n"
    ".Lmha_MSPIN:\n"
    "ds_read_b128 v[252:255], v238\n"
    "s_waitcnt lgkmcnt(0)\n"
    "v_min3_u32 v235, v252, v253, v254\n"
    "s_sub_u32 s79, s79, 1\n"
    "v_readfirstlane_b32 s72, v235\n"
    "s_max_i32 s79, s79, 0\n"
    "s_cbranch_scc0 .Lmha_MSPINX\n"
    "s_cmp_ge_u32 s72, s74\n"
    "s_cbranch_scc1 .Lmha_MSPINX\n"
    "s_sleep 1\n"
    "s_branch .Lmha_MSPIN\n"
    ".Lmha_MSPINX:\n"
    "s_setpc_b64 s[84:85]\n"
    : [tid] "+v"(tid)
    : [karg] "s"(__builtin_amdgcn_kernarg_segment_ptr()), [blk] "s"(blockIdx.x)
    : "v1", "v2", "v3", "v4", "v5", "v6", "v7", "v8", "v9", "v10", "v11", "v12", "v13", "v14", "v15", "v16", "v17", "v18", "v19", "v20", "v21", "v22", "v23", "v24", "v25", "v26", "v27", "v28", "v29", "v30", "v31", "v32", "v33", "v34", "v35", "v36", "v37", "v38", "v39", "v40", "v41", "v42", "v43", "v44", "v45", "v46", "v47", "v48", "v49", "v50", "v51", "v52", "v53", "v54", "v55", "v56", "v57", "v58", "v59", "v60", "v61", "v62", "v63", "v64", "v65", "v66", "v67", "v68", "v69", "v70", "v71", "v72", "v73", "v74", "v75", "v76", "v77", "v78", "v79", "v80", "v81", "v82", "v83", "v84", "v85", "v86", "v87", "v88", "v89", "v90", "v91", "v92", "v93", "v94", "v95", "v96", "v97", "v98", "v99", "v100", "v101", "v102", "v103", "v104", "v105", "v106", "v107", "v108", "v109", "v110", "v111", "v112", "v113", "v114", "v115", "v116", "v117", "v118", "v119", "v120", "v121", "v122", "v123", "v124", "v125", "v126", "v127", "v128", "v129", "v130", "v131", "v132", "v133", "v134", "v135", "v136", "v137", "v138", "v139", "v140", "v141", "v142", "v143", "v144", "v145", "v146", "v147", "v148", "v149", "v150", "v151", "v152", "v153", "v154", "v155", "v156", "v157", "v158", "v159", "v160", "v161", "v162", "v163", "v164", "v165", "v166", "v167", "v168", "v169", "v170", "v171", "v172", "v173", "v174", "v175", "v176", "v177", "v178", "v179", "v180", "v181", "v182", "v183", "v184", "v185", "v186", "v187", "v188", "v189", "v190", "v191", "v192", "v193", "v194", "v195", "v196", "v197", "v198", "v199", "v200", "v201", "v202", "v203", "v204", "v205", "v206", "v207", "v208", "v209", "v210", "v211", "v212", "v213", "v214", "v215", "v216", "v217", "v218", "v219", "v220", "v221", "v222", "v223", "v224", "v225", "v226", "v227", "v228", "v229", "v230", "v231", "v232", "v233", "v234", "v235", "v236", "v237", "v238", "v239", "v240", "v241", "v242", "v243", "v244", "v245", "v246", "v247", "v248", "v249", "v250", "v251", "v252", "v253", "v254", "v255", "a0", "a1", "a2", "a3", "a4", "a5", "a6", "a7", "a8", "a9", "a10", "a11", "a12", "a13", "a14", "a15", "a16", "a17", "a18", "a19", "a20", "a21", "a22", "a23", "a24", "a25", "a26", "a27", "a28", "a29", "a30", "a31", "a32", "a33", "a34", "a35", "a36", "a37", "a38", "a39", "a40", "a41", "a42", "a43", "a44", "a45", "a46", "a47", "a48", "a49", "a50", "a51", "a52", "a53", "a54", "a55", "a56", "a57", "a58", "a59", "a60", "a61", "a62", "a63", "a64", "a65", "a66", "a67", "a68", "a69", "a70", "a71", "a72", "a73", "a74", "a75", "a76", "a77", "a78", "a79", "a80", "a81", "a82", "a83", "a84", "a85", "a86", "a87", "a88", "a89", "a90", "a91", "a92", "a93", "a94", "a95", "a96", "a97", "a98", "a99", "a100", "a101", "a102", "a103", "a104", "a105", "a106", "a107", "a108", "a109", "a110", "a111", "a112", "a113", "a114", "a115", "a116", "a117", "a118", "a119", "a120", "a121", "a122", "a123", "a124", "a125", "a126", "a127", "a128", "a129", "a130", "a131", "a132", "a133", "a134", "a135", "a136", "a137", "a138", "a139", "a140", "a141", "a142", "a143", "a144", "a145", "a146", "a147", "a148", "a149", "a150", "a151", "a152", "a153", "a154", "a155", "a156", "a157", "a158", "a159", "a160", "a161", "a162", "a163", "a164", "a165", "a166", "a167", "a168", "a169", "a170", "a171", "a172", "a173", "a174", "a175", "a176", "a177", "a178", "a179", "a180", "a181", "a182", "a183", "a184", "a185", "a186", "a187", "a188", "a189", "a190", "a191", "a192", "a193", "a194", "a195", "a196", "a197", "a198", "a199", "a200", "a201", "a202", "a203", "a204", "a205", "a206", "a207", "a208", "a209", "a210", "a211", "a212", "a213", "a214", "a215", "a216", "a217", "a218", "a219", "a220", "a221", "a222", "a223", "a224", "a225", "a226", "a227", "a228", "a229", "a230", "a231", "a232", "a233", "a234", "a235", "a236", "a237", "a238", "a239", "a240", "a241", "a242", "a243", "a244", "a245", "a246", "a247", "a248", "a249", "a250", "a251", "a252", "a253", "a254", "a255", "s18", "s19", "s20", "s21", "s22", "s23", "s24", "s25", "s26", "s27", "s28", "s29", "s30", "s31", "s33", "s34", "s35", "s36", "s37", "s38", "s39", "s40", "s41", "s42", "s43", "s44", "s45", "s46", "s47", "s48", "s49", "s50", "s51", "s52", "s53", "s54", "s55", "s56", "s57", "s58", "s59", "s60", "s61", "s62", "s63", "s64", "s65", "s66", "s67", "s68", "s69", "s70", "s71", "s72", "s73", "s74", "s75", "s76", "s77", "s78", "s79", "s80", "s81", "s82", "s83", "s84", "s85", "s86", "s87", "s88", "s89", "s90", "s91", "s92", "s93", "s94", "s95", "s96", "s97", "s98", "s99", "vcc", "memory");
}

extern "C" void kernel_launch(void* const* d_in, const int* in_sizes, int n_in, void* d_out, int out_size, void* d_ws, size_t ws_size,
                              hipStream_t stream) {
  (void)hipFuncSetAttribute((const void*)mha_block_kernel, hipFuncAttributeMaxDynamicSharedMemorySize, LDS_BYTES);
  mha_block_kernel<<<256, 256, LDS_BYTES, stream>>>(d_in[0], d_in[1], d_in[2], d_in[3], d_in[4], d_out, d_ws,
                                                    (char*)d_ws + ws_size - (1 << 20));
}
```
